```python
import torch

def _softmax(x, axis=-1):
    return torch.softmax(x, dim=axis)

def _gelu(x):
    return torch.nn.functional.gelu(x, approximate='tanh')

def _silu(x):
    return torch.nn.functional.silu(x)

def _sigmoid(x):
    return torch.sigmoid(x)

def _layernorm(x, axis=-1, eps=1e-05):
    m = x.mean(dim=axis, keepdim=True)
    v = x.var(dim=axis, keepdim=True, unbiased=False)
    return (x - m) / torch.sqrt(v + eps)

def _scatter_add(tgt, idx, src, axis=0):
    return tgt.clone().index_add_(axis, idx, src)

def _segment_sum(src, seg, n):
    out = torch.zeros((n,) + src.shape[1:], dtype=src.dtype)
    return out.index_add_(0, seg, src)

def _pad(x, pw):
    flat = tuple((v for pair in reversed(pw) for v in pair))
    return torch.nn.functional.pad(x, flat)
kernel_name = 'standalone__video_timesformer__vision'

def setup_inputs():

    _w = lambda a: (lambda t: t.to(torch.bfloat16) if t.is_floating_point() else t)(torch.as_tensor(a))
    T = 7
    p = 14
    HW = 384 // p * p
    N = (HW // p) ** 2
    C = 384
    return {'x': _w((lambda *s: torch.randn(*s, dtype=torch.bfloat16) * 0.1)(1, T, N, C)), 'wqkv_t': _w((lambda *s: torch.randn(*s, dtype=torch.bfloat16) * 0.1)(C, 3 * C)), 'wo_t': _w((lambda *s: torch.randn(*s, dtype=torch.bfloat16) * 0.1)(C, C)), 'wqkv_s': _w((lambda *s: torch.randn(*s, dtype=torch.bfloat16) * 0.1)(C, 3 * C)), 'wo_s': _w((lambda *s: torch.randn(*s, dtype=torch.bfloat16) * 0.1)(C, C))}

def reference(x, wqkv_t, wo_t, wqkv_s, wo_s):
    B, T, N, C = x.shape
    scale = C ** (-0.5)
    xt = (lambda x, *d: x.permute(*d))(x, 0, 2, 1, 3).reshape(B * N, T, C)
    h = _layernorm(xt, axis=-1)
    qkv = h @ wqkv_t
    q, k, v = (qkv[..., :C].clone(), qkv[..., C:2 * C].clone(), qkv[..., 2 * C:].clone())
    a = _softmax(torch.einsum('btc,bsc->bts', q, k) * scale, axis=-1)
    xt = xt + torch.einsum('bts,bsc->btc', a, v) @ wo_t
    x1 = (lambda x, *d: x.permute(*d))(xt.reshape(B, N, T, C), 0, 2, 1, 3)
    xs = x1.reshape(B * T, N, C)
    h = _layernorm(xs, axis=-1)
    qkv = h @ wqkv_s
    q, k, v = (qkv[..., :C].clone(), qkv[..., C:2 * C].clone(), qkv[..., 2 * C:].clone())
    a = _softmax(torch.einsum('bnc,bmc->bnm', q, k) * scale, axis=-1)
    xs = xs + torch.einsum('bnm,bmc->bnc', a, v) @ wo_s
    return xs.reshape(B, T, N, C)
```

```cpp
#include <hip/hip_runtime.h>
#include <hip/hip_bf16.h>
#include <stdint.h>

typedef __attribute__((ext_vector_type(8))) __bf16 bf16x8;
typedef __attribute__((ext_vector_type(4))) __bf16 bf16x4;
typedef __attribute__((ext_vector_type(4))) float f32x4;
typedef __attribute__((ext_vector_type(4))) unsigned u32x4;
typedef __attribute__((ext_vector_type(2))) unsigned u32x2;
#define LDS_AS __attribute__((address_space(3)))
#define GLB_AS __attribute__((address_space(1)))
#define DI __device__ __forceinline__
#define MFMA16(a, b, c) __builtin_amdgcn_mfma_f32_16x16x32_bf16((a), (b), (c), 0, 0, 0)

namespace {

constexpr int NT = 7, NN = 729, NP = 736;
constexpr int ROWB = 768;
constexpr size_t WS_W1   = 0;
constexpr size_t WS_WVOS = 884736;
constexpr size_t WS_X1   = WS_WVOS + 294912;
constexpr size_t WS_H1   = WS_X1 + 3919872;
constexpr size_t WS_QS   = WS_H1 + 3956736;
constexpr float QSCALE = 0.05103103630798288f * 1.4426950408889634f;
constexpr float NEG = -1e30f;

DI int swz(int k) { return (k & 8) | ((k & 3) << 1); }
DI void lds_barrier() { asm volatile("s_waitcnt lgkmcnt(0)" ::: "memory"); __builtin_amdgcn_s_barrier(); asm volatile("" ::: "memory"); }
DI bf16x8 ld16(const void* p) { return *(const bf16x8*)p; }
DI bf16x4 pack4(f32x4 v) { bf16x4 r = {(__bf16)v[0], (__bf16)v[1], (__bf16)v[2], (__bf16)v[3]}; return r; }
DI bf16x8 cat8(bf16x4 a, bf16x4 b) { return __builtin_shufflevector(a, b, 0, 1, 2, 3, 4, 5, 6, 7); }
DI bf16x4 tr_read(const char* p) { return __builtin_amdgcn_ds_read_tr16_b64_v4bf16((LDS_AS bf16x4*)p); }
DI float red16_sum(float v) { v += __shfl_xor(v, 1); v += __shfl_xor(v, 2); v += __shfl_xor(v, 4); v += __shfl_xor(v, 8); return v; }

DI void ln_row(const bf16x8 (&xin)[3], bf16x8 (&hout)[3]) {
  float v[24]; float s = 0.f;
#pragma unroll
  for (int i = 0; i < 3; ++i)
#pragma unroll
    for (int j = 0; j < 8; ++j) { v[i * 8 + j] = (float)xin[i][j]; s += v[i * 8 + j]; }
  const float mean = red16_sum(s) * (1.f / 384.f);
  float q = 0.f;
#pragma unroll
  for (int k = 0; k < 24; ++k) { v[k] -= mean; q += v[k] * v[k]; }
  const float rstd = rsqrtf(red16_sum(q) * (1.f / 384.f) + 1e-5f);
#pragma unroll
  for (int i = 0; i < 3; ++i)
#pragma unroll
    for (int j = 0; j < 8; ++j) hout[i][j] = (__bf16)(v[i * 8 + j] * rstd);
}

__global__ __launch_bounds__(256) void k0_fold(const char* __restrict__ wqkv_t, const char* __restrict__ wo_t,
                                               const char* __restrict__ wqkv_s, const char* __restrict__ wo_s,
                                               char* __restrict__ ws) {
  __shared__ __attribute__((aligned(16))) char sm[384 * 112];
  const int b = blockIdx.x, mat = b >> 6, ti = (b >> 3) & 7, tj = b & 7;
  const int tid = threadIdx.x, wid = __builtin_amdgcn_readfirstlane(tid >> 6), lane = tid & 63, g = lane >> 4, r16 = lane & 15;
  const char* wqkv = mat < 2 ? wqkv_t : wqkv_s;
  const char* wo = mat < 2 ? wo_t : wo_s;
  const bool isqk = (mat & 1) == 0;
  bf16x8 a[12];
  if (wid < 3) {
    const char* ap = wqkv + ((48 * ti + 16 * wid + r16) * 1152 + (isqk ? 0 : 768) + 8 * g) * 2;
#pragma unroll
    for (int ks = 0; ks < 12; ++ks) a[ks] = ld16(ap + ks * 64);
  }
  if (isqk) {
#pragma unroll
    for (int it = 0; it < 9; ++it) {
      const int idx = tid + 256 * it, row = idx / 48, ch = idx % 48;
      *(bf16x8*)(sm + row * 768 + 16 * (ch ^ swz(row & 15))) = ld16(wqkv + ((48 * tj + row) * 1152 + 384 + ch * 8) * 2);
    }
  } else {
#pragma unroll
    for (int it = 0; it < 9; ++it) {
      const int idx = tid + 256 * it, row = idx / 6, ch = idx % 6;
      *(bf16x8*)(sm + row * 112 + ch * 16) = ld16(wo + (row * 384 + 48 * tj + ch * 8) * 2);
    }
  }
  if (wid == 3 && b < 14) {
    char* dst = ws + (b < 7 ? WS_H1 : WS_QS) + ((size_t)(b % 7) * NP + NN) * ROWB;
    const u32x4 z = {0u, 0u, 0u, 0u};
    for (int i = lane; i < 7 * 48; i += 64) *(u32x4*)(dst + i * 16) = z;
  }
  __syncthreads();
  if (wid < 3) {
    f32x4 acc[3] = {};
    if (isqk) {
#pragma unroll
      for (int ks = 0; ks < 12; ++ks)
#pragma unroll
        for (int jt = 0; jt < 3; ++jt)
          acc[jt] = MFMA16(a[ks], *(const bf16x8*)(sm + (16 * jt + r16) * 768 + 16 * ((4 * ks + g) ^ swz(r16))), acc[jt]);
    } else {
      const char* tb = sm + (8 * g + (r16 >> 2)) * 112 + (lane & 3) * 8;
#pragma unroll
      for (int ks = 0; ks < 12; ++ks)
#pragma unroll
        for (int jt = 0; jt < 3; ++jt) {
          const bf16x4 lo = tr_read(tb + (32 * ks) * 112 + jt * 32), hi = tr_read(tb + (32 * ks + 4) * 112 + jt * 32);
          acc[jt] = MFMA16(a[ks], cat8(lo, hi), acc[jt]);
        }
    }
    const float sc = isqk ? QSCALE : 1.f;
    const int IT = 3 * ti + wid, ks = IT >> 1, gp = 2 * (IT & 1) + (g >> 1);
#pragma unroll
    for (int jt = 0; jt < 3; ++jt) {
      const int JT = 3 * tj + jt;
      size_t off;
      if (mat == 3) off = WS_WVOS + (size_t)(ks * 24 + JT) * 1024;
      else off = WS_W1 + (size_t)((JT / 6) * 216 + mat * 72 + (JT % 6) * 12 + ks) * 1024;
      *(bf16x4*)(ws + off + (r16 + 16 * gp) * 16 + 8 * (g & 1)) = pack4(acc[jt] * sc);
    }
  }
}

constexpr int K1_D = 24;
constexpr int MIXB = 1552;
__global__ __launch_bounds__(256, 1) void k1_temporal(const char* __restrict__ x, char* __restrict__ ws) {
  __shared__ __attribute__((aligned(16))) char sm[24576 + 32 * MIXB + 8192];
  char* const Ht = sm; char* const Mx = sm + 24576; char* const Sp = sm + 24576 + 32 * MIXB;
  const int tid = threadIdx.x, wid = __builtin_amdgcn_readfirstlane(tid >> 6), lane = tid & 63, g = lane >> 4, r16 = lane & 15;
  const int n0 = blockIdx.x * 4, n = n0 + wid;
  const char* wp = ws + WS_W1 + (size_t)wid * 216 * 1024 + lane * 16;

  bf16x8 xr[2][3];
#pragma unroll
  for (int p = 0; p < 2; ++p) {
    const int t = 4 * p + g;
    const bool ok = (n < NN) && (t < NT);
    const char* xp = x + ((size_t)(ok ? t * NN + n : 0)) * ROWB + r16 * 16;
#pragma unroll
    for (int i = 0; i < 3; ++i) { xr[p][i] = ld16(xp + i * 256); if (!ok) xr[p][i] = bf16x8{}; }
  }
  bf16x8 w[K1_D];
#pragma unroll
  for (int s = 0; s < K1_D; ++s) w[s] = ld16(wp + s * 1024);

#pragma unroll
  for (int p = 0; p < 2; ++p) {
    bf16x8 h[3]; ln_row(xr[p], h);
    const int slot = 8 * wid + 4 * p + g;
#pragma unroll
    for (int i = 0; i < 3; ++i) *(bf16x8*)(Ht + slot * ROWB + 16 * ((r16 + 16 * i) ^ swz(slot & 15))) = h[i];
  }
  lds_barrier();
  bf16x8 hf[2][12];
#pragma unroll
  for (int u = 0; u < 2; ++u)
#pragma unroll
    for (int ks = 0; ks < 12; ++ks) hf[u][ks] = *(const bf16x8*)(Ht + (16 * u + r16) * ROWB + 16 * ((4 * ks + g) ^ swz(r16)));

  f32x4 acc[6][2];
#pragma unroll
  for (int c6 = 0; c6 < 6; ++c6) {
    acc[c6][0] = f32x4{}; acc[c6][1] = f32x4{};
#pragma unroll
    for (int ks = 0; ks < 12; ++ks) {
      const int s = c6 * 12 + ks;
      const bf16x8 wf = w[s % K1_D];
      w[s % K1_D] = ld16(wp + (s + K1_D) * 1024);
      acc[c6][0] = MFMA16(wf, hf[0][ks], acc[c6][0]);
      acc[c6][1] = MFMA16(wf, hf[1][ks], acc[c6][1]);
    }
  }
  {
    f32x4 sacc[2] = {};
#pragma unroll
    for (int kk = 0; kk < 3; ++kk)
#pragma unroll
      for (int u = 0; u < 2; ++u) {
        const char* hp = Ht + (16 * u + r16) * ROWB + 8 * (g & 1);
        const int ch = 12 * wid + 4 * kk + (g >> 1);
        const bf16x4 lo = *(const bf16x4*)(hp + 16 * (ch ^ swz(r16))), hi = *(const bf16x4*)(hp + 16 * ((ch + 2) ^ swz(r16)));
        sacc[u] = MFMA16(cat8(lo, hi), cat8(pack4(acc[2 * kk][u]), pack4(acc[2 * kk + 1][u])), sacc[u]);
      }
    *(f32x4*)(Sp + (wid * 2 + 0) * 1024 + lane * 16) = sacc[0];
    *(f32x4*)(Sp + (wid * 2 + 1) * 1024 + lane * 16) = sacc[1];
  }
#pragma unroll
  for (int c6 = 0; c6 < 6; ++c6) {
    acc[c6][0] = f32x4{}; acc[c6][1] = f32x4{};
#pragma unroll
    for (int ks = 0; ks < 12; ++ks) {
      const int s = 72 + c6 * 12 + ks;
      const bf16x8 wf = w[s % K1_D];
      w[s % K1_D] = ld16(wp + (s + K1_D) * 1024);
      acc[c6][0] = MFMA16(hf[0][ks], wf, acc[c6][0]);
      acc[c6][1] = MFMA16(hf[1][ks], wf, acc[c6][1]);
    }
  }
  lds_barrier();
  {
    bf16x8 pf[2];
#pragma unroll
    for (int u = 0; u < 2; ++u) {
      f32x4 s = *(const f32x4*)(Sp + u * 1024 + lane * 16);
#pragma unroll
      for (int ww = 1; ww < 4; ++ww) s += *(const f32x4*)(Sp + (ww * 2 + u) * 1024 + lane * 16);
      const bool blk = (g >> 1) == (r16 >> 3);
      bool ok[4]; float mx = NEG;
#pragma unroll
      for (int r = 0; r < 4; ++r) { ok[r] = blk && (4 * (g & 1) + r < NT); mx = fmaxf(mx, ok[r] ? s[r] : NEG); }
      mx = fmaxf(mx, __shfl_xor(mx, 16));
      f32x4 p; float sum = 0.f;
#pragma unroll
      for (int r = 0; r < 4; ++r) { p[r] = ok[r] ? __builtin_amdgcn_exp2f(s[r] - mx) : 0.f; sum += p[r]; }
      sum += __shfl_xor(sum, 16);
      const float inv = blk ? 1.f / sum : 0.f;
      const bf16x4 pb = pack4(p * inv), z = {};
      pf[u] = u == 0 ? cat8(pb, z) : cat8(z, pb);
    }
#pragma unroll
    for (int c6 = 0; c6 < 6; ++c6) {
      const bf16x8 vf = cat8(pack4(acc[c6][0]), pack4(acc[c6][1]));
#pragma unroll
      for (int u = 0; u < 2; ++u)
        *(f32x4*)(Mx + (16 * u + r16) * MIXB + (96 * wid + 16 * c6 + 4 * g) * 4) = MFMA16(vf, pf[u], f32x4{});
    }
  }
  lds_barrier();
#pragma unroll
  for (int p = 0; p < 2; ++p) {
    const int t = 4 * p + g, slot = 8 * wid + t;
    const bool ok = (n < NN) && (t < NT);
    bf16x8 x1[3], h[3];
#pragma unroll
    for (int i = 0; i < 3; ++i) {
      const f32x4 m0 = *(const f32x4*)(Mx + slot * MIXB + (r16 + 16 * i) * 32), m1 = *(const f32x4*)(Mx + slot * MIXB + (r16 + 16 * i) * 32 + 16);
#pragma unroll
      for (int j = 0; j < 4; ++j) { x1[i][j] = (__bf16)((float)xr[p][i][j] + m0[j]); x1[i][4 + j] = (__bf16)((float)xr[p][i][4 + j] + m1[j]); }
    }
    ln_row(x1, h);
#pragma unroll
    for (int i = 0; i < 3; ++i) *(bf16x8*)(Ht + slot * ROWB + 16 * ((r16 + 16 * i) ^ swz(slot & 15))) = h[i];
    if (ok) {
      char* xo = ws + WS_X1 + (size_t)(t * NN + n) * ROWB + r16 * 16;
      char* ho = ws + WS_H1 + (size_t)(t * NP + n) * ROWB;
#pragma unroll
      for (int i = 0; i < 3; ++i) { *(bf16x8*)(xo + i * 256) = x1[i]; *(bf16x8*)(ho + 16 * ((r16 + 16 * i) ^ swz(n & 15))) = h[i]; }
    }
  }
  lds_barrier();
#pragma unroll
  for (int u = 0; u < 2; ++u)
#pragma unroll
    for (int ks = 0; ks < 12; ++ks) hf[u][ks] = *(const bf16x8*)(Ht + (16 * u + r16) * ROWB + 16 * ((4 * ks + g) ^ swz(r16)));
#pragma unroll
  for (int c6 = 0; c6 < 6; ++c6) {
    acc[c6][0] = f32x4{}; acc[c6][1] = f32x4{};
#pragma unroll
    for (int ks = 0; ks < 12; ++ks) {
      const int s = 144 + c6 * 12 + ks;
      const bf16x8 wf = w[s % K1_D];
      if (s + K1_D < 216) w[s % K1_D] = ld16(wp + (s + K1_D) * 1024);
      acc[c6][0] = MFMA16(wf, hf[0][ks], acc[c6][0]);
      acc[c6][1] = MFMA16(wf, hf[1][ks], acc[c6][1]);
    }
#pragma unroll
    for (int u = 0; u < 2; ++u) {
      const int tok = 16 * u + r16, t = tok & 7, nq = n0 + (tok >> 3);
      if (t < NT && nq < NN)
        *(bf16x4*)(ws + WS_QS + (size_t)(t * NP + nq) * ROWB + 16 * ((12 * wid + 2 * c6 + (g >> 1)) ^ swz(nq & 15)) + 8 * (g & 1)) = pack4(acc[c6][u]);
    }
  }
}

constexpr int SLOT = 24576;
constexpr int K2_P = 6 * SLOT;
constexpr int K2_MX = K2_P + 8192;
constexpr int K2_L = K2_MX + 1024;
constexpr int K2_LDS = K2_L + 512;

DI void k2_issue(const char* src, char* sm, int item, int lw, int lane) {
  const char* s = src + lw * 6144 + lane * 16;
  char* d = sm + (item % 6) * SLOT + lw * 6144;
#pragma unroll
  for (int i = 0; i < 6; ++i)
    __builtin_amdgcn_global_load_lds((const GLB_AS void*)(s + i * 1024), (LDS_AS void*)(d + i * 1024), 16, 0, 0);
}
#define VMWAIT(n) asm volatile("s_waitcnt vmcnt(" #n ")" ::: "memory")
DI void bar() { __builtin_amdgcn_s_barrier(); asm volatile("" ::: "memory"); }

__global__ __launch_bounds__(512, 2) void k2_spatial(const char* __restrict__ ws, char* __restrict__ out) {
  extern __shared__ __attribute__((aligned(16))) char sm[];
  const int f = blockIdx.x & 7, qt = blockIdx.x >> 3;
  if (f >= NT) return;
  const int tid = threadIdx.x, wid = __builtin_amdgcn_readfirstlane(tid >> 6), lane = tid & 63, g = lane >> 4, r16 = lane & 15;

  if (wid >= 4) {
    const int lw = wid - 4;
    const char* kv = ws + WS_H1 + (size_t)f * NP * ROWB;
    const char* wv = ws + WS_WVOS;
    k2_issue(ws + WS_QS + (size_t)(f * NP + 32 * qt) * ROWB, sm, 0, lw, lane);
#pragma unroll
    for (int it = 1; it < 6; ++it) k2_issue(kv + (it - 1) * SLOT, sm, it, lw, lane);
    VMWAIT(18); bar();
    bar(); k2_issue(kv + 5 * SLOT, sm, 6, lw, lane);
    VMWAIT(12); bar();
    for (int i = 1; i <= 8; ++i) {
      bar(); k2_issue(kv + (2 * i + 4) * SLOT, sm, 2 * i + 5, lw, lane);
      VMWAIT(6); bar(); k2_issue(kv + (2 * i + 5) * SLOT, sm, 2 * i + 6, lw, lane);
    }
    bar(); k2_issue(kv + 22 * SLOT, sm, 23, lw, lane);
    VMWAIT(6); bar();
    bar(); k2_issue(wv, sm, 25, lw, lane);
    VMWAIT(6); bar(); k2_issue(wv + SLOT, sm, 26, lw, lane);
    bar(); k2_issue(wv + 2 * SLOT, sm, 27, lw, lane);
    bar(); k2_issue(wv + 3 * SLOT, sm, 28, lw, lane);
    bar(); k2_issue(wv + 4 * SLOT, sm, 29, lw, lane);
    VMWAIT(18); bar();
    VMWAIT(6); bar();
    k2_issue(wv + 5 * SLOT, sm, 30, lw, lane); k2_issue(wv + 6 * SLOT, sm, 31, lw, lane); k2_issue(wv + 7 * SLOT, sm, 32, lw, lane);
    VMWAIT(12); bar();
    k2_issue(wv + 8 * SLOT, sm, 33, lw, lane); k2_issue(wv + 9 * SLOT, sm, 34, lw, lane);
    VMWAIT(12); bar();
    k2_issue(wv + 10 * SLOT, sm, 35, lw, lane); k2_issue(wv + 11 * SLOT, sm, 36, lw, lane);
    VMWAIT(12); bar();
    VMWAIT(0); bar();
    return;
  }

  const int fsw = swz(r16);
  int roff[4];
#pragma unroll
  for (int b = 0; b < 4; ++b) roff[b] = r16 * ROWB + 16 * ((4 * b + g) ^ fsw);
  const int vsw = (8 * (g & 1)) | ((r16 >> 2) << 1);
  int voff[6];
#pragma unroll
  for (int i = 0; i < 6; ++i) voff[i] = (8 * g + (r16 >> 2)) * ROWB + 16 * ((12 * wid + 2 * i + ((lane & 3) >> 1)) ^ vsw) + 8 * (lane & 1);
  const int pw_off = r16 * 128 + 16 * ((2 * wid + (g >> 1)) ^ (r16 & 7)) + 8 * (g & 1);
  int pr_off[2];
#pragma unroll
  for (int hh = 0; hh < 2; ++hh) pr_off[hh] = r16 * 128 + 16 * ((4 * hh + g) ^ (r16 & 7));

  bf16x8 qf[2][12];
  f32x4 o[6][2];
#pragma unroll
  for (int i = 0; i < 6; ++i) { o[i][0] = f32x4{}; o[i][1] = f32x4{}; }
  float m[2] = {NEG, NEG}, l[2] = {0.f, 0.f}, alpha[2] = {1.f, 1.f};
  f32x4 s[2];

  auto qk = [&](int i) {
    const char* kb = sm + ((2 * i + 1 + (wid >> 1)) % 6) * SLOT + (wid & 1) * 16 * ROWB;
    s[0] = f32x4{}; s[1] = f32x4{};
    if (i < 11 || wid < 2) {
#pragma unroll
      for (int ks = 0; ks < 12; ++ks) {
        const bf16x8 kf = *(const bf16x8*)(kb + roff[ks & 3] + (ks >> 2) * 256);
        s[0] = MFMA16(kf, qf[0][ks], s[0]);
        s[1] = MFMA16(kf, qf[1][ks], s[1]);
      }
    }
    if (i == 11) {
#pragma unroll
      for (int r = 0; r < 4; ++r)
        if (704 + 16 * wid + 4 * g + r >= NN) { s[0][r] = NEG; s[1][r] = NEG; }
    }
#pragma unroll
    for (int u = 0; u < 2; ++u) {
      float mx = fmaxf(fmaxf(s[u][0], s[u][1]), fmaxf(s[u][2], s[u][3]));
      mx = fmaxf(mx, __shfl_xor(mx, 16)); mx = fmaxf(mx, __shfl_xor(mx, 32));
      if (g == 0) *(float*)(sm + K2_MX + (i & 1) * 512 + (16 * u + r16) * 16 + wid * 4) = mx;
    }
  };
  auto softmax = [&](int i) {
#pragma unroll
    for (int u = 0; u < 2; ++u) {
      const f32x4 mm = *(const f32x4*)(sm + K2_MX + (i & 1) * 512 + (16 * u + r16) * 16);
      const float mn = fmaxf(fmaxf(fmaxf(mm[0], mm[1]), fmaxf(mm[2], mm[3])), m[u]);
      alpha[u] = __builtin_amdgcn_exp2f(m[u] - mn);
      m[u] = mn;
      f32x4 p; float sum = 0.f;
#pragma unroll
      for (int r = 0; r < 4; ++r) { p[r] = __builtin_amdgcn_exp2f(s[u][r] - mn); sum += p[r]; }
      l[u] = l[u] * alpha[u] + sum;
      *(bf16x4*)(sm + K2_P + (i & 1) * 4096 + u * 2048 + pw_off) = pack4(p);
    }
  };
  auto rescale = [&]() {
#pragma unroll
    for (int i = 0; i < 6; ++i) { o[i][0] *= alpha[0]; o[i][1] *= alpha[1]; }
  };
  auto pv = [&](int i, int hh) {
    const char* vb = sm + ((2 * i + 1 + hh) % 6) * SLOT;
    const char* pb = sm + K2_P + (i & 1) * 4096 + pr_off[hh];
    const bf16x8 p0 = *(const bf16x8*)pb, p1 = *(const bf16x8*)(pb + 2048);
#pragma unroll
    for (int c = 0; c < 6; ++c) {
      const bf16x8 vf = cat8(tr_read(vb + voff[c]), tr_read(vb + voff[c] + 4 * ROWB));
      o[c][0] = MFMA16(vf, p0, o[c][0]);
      o[c][1] = MFMA16(vf, p1, o[c][1]);
    }
  };

  bar();
#pragma unroll
  for (int u = 0; u < 2; ++u)
#pragma unroll
    for (int ks = 0; ks < 12; ++ks) qf[u][ks] = *(const bf16x8*)(sm + u * 16 * ROWB + roff[ks & 3] + (ks >> 2) * 256);
  qk(0);
  lds_barrier();
  softmax(0);
  for (int i = 1; i <= 11; ++i) {
    lds_barrier();
    rescale();
    qk(i);
    pv(i - 1, 0);
    lds_barrier();
    pv(i - 1, 1);
    softmax(i);
  }
  lds_barrier();
  rescale();
  pv(11, 0);
#pragma unroll
  for (int u = 0; u < 2; ++u) {
    float t = l[u]; t += __shfl_xor(t, 16); t += __shfl_xor(t, 32);
    if (g == 0) *(float*)(sm + K2_L + (16 * u + r16) * 16 + wid * 4) = t;
  }
  bf16x4 xres[6][2];
#pragma unroll
  for (int u = 0; u < 2; ++u) {
    const int q = 32 * qt + 16 * u + r16;
    const char* xp = ws + WS_X1 + (size_t)(f * NN + (q < NN ? q : 0)) * ROWB + (96 * wid + 4 * g) * 2;
#pragma unroll
    for (int c = 0; c < 6; ++c) xres[c][u] = *(const bf16x4*)(xp + c * 32);
  }
  lds_barrier();
#pragma unroll
  for (int u = 0; u < 2; ++u) {
    const f32x4 ll = *(const f32x4*)(sm + K2_L + (16 * u + r16) * 16);
    const float inv = 1.f / (ll[0] + ll[1] + ll[2] + ll[3]);
#pragma unroll
    for (int c = 0; c < 6; ++c)
      *(bf16x4*)(sm + (16 * u + r16) * ROWB + 16 * ((12 * wid + 2 * c + (g >> 1)) ^ fsw) + 8 * (g & 1)) = pack4(o[c][u] * inv);
  }
  lds_barrier();
#pragma unroll
  for (int u = 0; u < 2; ++u)
#pragma unroll
    for (int ks = 0; ks < 12; ++ks) qf[u][ks] = *(const bf16x8*)(sm + u * 16 * ROWB + roff[ks & 3] + (ks >> 2) * 256);
#pragma unroll
  for (int i = 0; i < 6; ++i) { o[i][0] = f32x4{}; o[i][1] = f32x4{}; }
#pragma unroll
  for (int e = 0; e < 6; ++e) {
    if (e > 0) lds_barrier();
#pragma unroll
    for (int k2 = 0; k2 < 2; ++k2) {
      const int ks = 2 * e + k2;
      const char* wb = sm + ((25 + ks) % 6) * SLOT + wid * 6144 + lane * 16;
#pragma unroll
      for (int c = 0; c < 6; ++c) {
        const bf16x8 wf = *(const bf16x8*)(wb + c * 1024);
        o[c][0] = MFMA16(wf, qf[0][ks], o[c][0]);
        o[c][1] = MFMA16(wf, qf[1][ks], o[c][1]);
      }
    }
  }
#pragma unroll
  for (int u = 0; u < 2; ++u) {
    const int q = 32 * qt + 16 * u + r16;
    if (q < NN) {
      char* op = out + (size_t)(f * NN + q) * ROWB + (96 * wid + 4 * g) * 2;
#pragma unroll
      for (int c = 0; c < 6; ++c) {
        f32x4 v = o[c][u];
#pragma unroll
        for (int r = 0; r < 4; ++r) v[r] += (float)xres[c][u][r];
        *(bf16x4*)(op + c * 32) = pack4(v);
      }
    }
  }
}

}

extern "C" void kernel_launch(void* const* d_in, const int* in_sizes, int n_in,
                              void* d_out, int out_size, void* d_ws, size_t ws_size,
                              hipStream_t stream) {
  (void)in_sizes; (void)n_in; (void)out_size; (void)ws_size;
  const char* x = (const char*)d_in[0];
  char* ws = (char*)d_ws;
  hipFuncSetAttribute((const void*)k2_spatial, hipFuncAttributeMaxDynamicSharedMemorySize, K2_LDS);
  k0_fold<<<256, 256, 0, stream>>>((const char*)d_in[1], (const char*)d_in[2], (const char*)d_in[3], (const char*)d_in[4], ws);
  k1_temporal<<<183, 256, 0, stream>>>(x, ws);
  k2_spatial<<<184, 512, K2_LDS, stream>>>(ws, (char*)d_out);
}
```

```cpp
#include <hip/hip_runtime.h>
#include <hip/hip_bf16.h>
#include <stdint.h>

typedef __attribute__((ext_vector_type(8))) __bf16 bf16x8;
typedef __attribute__((ext_vector_type(4))) __bf16 bf16x4;
typedef __attribute__((ext_vector_type(4))) float f32x4;
typedef __attribute__((ext_vector_type(4))) unsigned u32x4;
typedef __attribute__((ext_vector_type(2))) unsigned u32x2;
#define LDS_AS __attribute__((address_space(3)))
#define GLB_AS __attribute__((address_space(1)))
#define DI __device__ __forceinline__
#define MFMA16(a, b, c) __builtin_amdgcn_mfma_f32_16x16x32_bf16((a), (b), (c), 0, 0, 0)

namespace {

constexpr int NT = 7, NN = 729, NP = 736;
constexpr int ROWB = 768;
constexpr size_t WS_W1   = 0;
constexpr size_t WS_WVOS = 884736;
constexpr size_t WS_X1   = WS_WVOS + 294912;
constexpr size_t WS_H1   = WS_X1 + 3919872;
constexpr size_t WS_QS   = WS_H1 + 3956736;
constexpr float QSCALE = 0.05103103630798288f * 1.4426950408889634f;
constexpr float NEG = -1e30f;

DI int swz(int k) { return (k & 8) | ((k & 3) << 1); }
DI void lds_barrier() { asm volatile("s_waitcnt lgkmcnt(0)" ::: "memory"); __builtin_amdgcn_s_barrier(); asm volatile("" ::: "memory"); }
DI bf16x8 ld16(const void* p) { return *(const bf16x8*)p; }
DI bf16x4 pack4(f32x4 v) { bf16x4 r = {(__bf16)v[0], (__bf16)v[1], (__bf16)v[2], (__bf16)v[3]}; return r; }
DI bf16x8 cat8(bf16x4 a, bf16x4 b) { return __builtin_shufflevector(a, b, 0, 1, 2, 3, 4, 5, 6, 7); }
DI bf16x4 tr_read(const char* p) { return __builtin_amdgcn_ds_read_tr16_b64_v4bf16((LDS_AS bf16x4*)p); }
template <int CTRL> DI float dpp_mov(float v) { return __int_as_float(__builtin_amdgcn_update_dpp(0, __float_as_int(v), CTRL, 0xf, 0xf, false)); }
DI float red16_sum(float v) {
  v += dpp_mov<0x128>(v); v += dpp_mov<0x124>(v); v += dpp_mov<0x122>(v); v += dpp_mov<0x121>(v); return v;
}
DI float xmax16(float v) { auto r = __builtin_amdgcn_permlane16_swap(__float_as_uint(v), __float_as_uint(v), false, false); return fmaxf(__uint_as_float(r[0]), __uint_as_float(r[1])); }
DI float xmax32(float v) { auto r = __builtin_amdgcn_permlane32_swap(__float_as_uint(v), __float_as_uint(v), false, false); return fmaxf(__uint_as_float(r[0]), __uint_as_float(r[1])); }
DI float xsum16(float v) { auto r = __builtin_amdgcn_permlane16_swap(__float_as_uint(v), __float_as_uint(v), false, false); return __uint_as_float(r[0]) + __uint_as_float(r[1]); }
DI float xsum32(float v) { auto r = __builtin_amdgcn_permlane32_swap(__float_as_uint(v), __float_as_uint(v), false, false); return __uint_as_float(r[0]) + __uint_as_float(r[1]); }
#define SB0() __builtin_amdgcn_sched_barrier(0)

DI void ln_row(const bf16x8 (&xin)[3], bf16x8 (&hout)[3]) {
  f32x4 v[6];
#pragma unroll
  for (int i = 0; i < 3; ++i)
#pragma unroll
    for (int j = 0; j < 4; ++j) { v[2 * i][j] = (float)xin[i][j]; v[2 * i + 1][j] = (float)xin[i][4 + j]; }
  const f32x4 s4 = ((v[0] + v[1]) + (v[2] + v[3])) + (v[4] + v[5]);
  const float mean = red16_sum((s4[0] + s4[1]) + (s4[2] + s4[3])) * (1.f / 384.f);
  f32x4 q4 = {};
#pragma unroll
  for (int k = 0; k < 6; ++k) { v[k] -= mean; q4 += v[k] * v[k]; }
  const float rstd = rsqrtf(red16_sum((q4[0] + q4[1]) + (q4[2] + q4[3])) * (1.f / 384.f) + 1e-5f);
#pragma unroll
  for (int i = 0; i < 3; ++i) hout[i] = cat8(pack4(v[2 * i] * rstd), pack4(v[2 * i + 1] * rstd));
}

__global__ __launch_bounds__(256) void k0_fold(const char* __restrict__ wqkv_t, const char* __restrict__ wo_t,
                                               const char* __restrict__ wqkv_s, const char* __restrict__ wo_s,
                                               char* __restrict__ ws) {
  __shared__ __attribute__((aligned(16))) char sm[384 * 112];
  const int b = blockIdx.x, mat = b >> 6, ti = (b >> 3) & 7, tj = b & 7;
  const int tid = threadIdx.x, wid = __builtin_amdgcn_readfirstlane(tid >> 6), lane = tid & 63, g = lane >> 4, r16 = lane & 15;
  const char* wqkv = mat < 2 ? wqkv_t : wqkv_s;
  const char* wo = mat < 2 ? wo_t : wo_s;
  const bool isqk = (mat & 1) == 0;
  bf16x8 a[12];
  if (wid < 3) {
    const char* ap = wqkv + ((48 * ti + 16 * wid + r16) * 1152 + (isqk ? 0 : 768) + 8 * g) * 2;
#pragma unroll
    for (int ks = 0; ks < 12; ++ks) a[ks] = ld16(ap + ks * 64);
  }
  if (isqk) {
#pragma unroll
    for (int it = 0; it < 9; ++it) {
      const int idx = tid + 256 * it, row = idx / 48, ch = idx % 48;
      *(bf16x8*)(sm + row * 768 + 16 * (ch ^ swz(row & 15))) = ld16(wqkv + ((48 * tj + row) * 1152 + 384 + ch * 8) * 2);
    }
  } else {
#pragma unroll
    for (int it = 0; it < 9; ++it) {
      const int idx = tid + 256 * it, row = idx / 6, ch = idx % 6;
      *(bf16x8*)(sm + row * 112 + ch * 16) = ld16(wo + (row * 384 + 48 * tj + ch * 8) * 2);
    }
  }
  if (wid == 3 && b < 14) {
    char* dst = ws + (b < 7 ? WS_H1 : WS_QS) + ((size_t)(b % 7) * NP + NN) * ROWB;
    const u32x4 z = {0u, 0u, 0u, 0u};
    for (int i = lane; i < 7 * 48; i += 64) *(u32x4*)(dst + i * 16) = z;
  }
  __syncthreads();
  if (wid < 3) {
    f32x4 acc[3] = {};
    if (isqk) {
#pragma unroll
      for (int ks = 0; ks < 12; ++ks)
#pragma unroll
        for (int jt = 0; jt < 3; ++jt)
          acc[jt] = MFMA16(a[ks], *(const bf16x8*)(sm + (16 * jt + r16) * 768 + 16 * ((4 * ks + g) ^ swz(r16))), acc[jt]);
    } else {
      const char* tb = sm + (8 * g + (r16 >> 2)) * 112 + (lane & 3) * 8;
#pragma unroll
      for (int ks = 0; ks < 12; ++ks)
#pragma unroll
        for (int jt = 0; jt < 3; ++jt) {
          const bf16x4 lo = tr_read(tb + (32 * ks) * 112 + jt * 32), hi = tr_read(tb + (32 * ks + 4) * 112 + jt * 32);
          acc[jt] = MFMA16(a[ks], cat8(lo, hi), acc[jt]);
        }
    }
    const float sc = isqk ? QSCALE : 1.f;
    const int IT = 3 * ti + wid, ks = IT >> 1, gp = 2 * (IT & 1) + (g >> 1);
#pragma unroll
    for (int jt = 0; jt < 3; ++jt) {
      const int JT = 3 * tj + jt;
      size_t off;
      if (mat == 3) off = WS_WVOS + (size_t)(ks * 24 + JT) * 1024;
      else off = WS_W1 + (size_t)(tj * 108 + mat * 36 + ks * 3 + jt) * 1024;
      *(bf16x4*)(ws + off + (r16 + 16 * gp) * 16 + 8 * (g & 1)) = pack4(acc[jt] * sc);
    }
  }
}

constexpr int K1_D = 30;
constexpr int MIXB = 1552;
__global__ __launch_bounds__(512, 2) void k1_temporal(const char* __restrict__ x, char* __restrict__ ws) {
  __shared__ __attribute__((aligned(16))) char sm[24576 + 32 * MIXB + 16384];
  char* const Ht = sm; char* const Mx = sm + 24576; char* const Sp = sm + 24576 + 32 * MIXB;
  const int tid = threadIdx.x, wid = __builtin_amdgcn_readfirstlane(tid >> 6), lane = tid & 63, g = lane >> 4, r16 = lane & 15;
  const int n0 = blockIdx.x * 4, n = n0 + (wid >> 1);
  const int slot = 4 * wid + g, t = slot & 7;
  const bool ok = (n < NN) && (t < NT);
  bf16x8 xr[3];
  {
    const char* xp = x + ((size_t)(ok ? t * NN + n : 0)) * ROWB + r16 * 16;
#pragma unroll
    for (int i = 0; i < 3; ++i) xr[i] = ld16(xp + i * 256);
  }
  SB0();
  const char* wp = ws + WS_W1 + (size_t)wid * 108 * 1024 + lane * 16;
  const int rot = (blockIdx.x >> 3) % 12, rot2 = ((blockIdx.x >> 3) / 12 + blockIdx.x) % 3;
  int kso[12], c3o[3];
#pragma unroll
  for (int j = 0; j < 12; ++j) kso[j] = (j + rot) % 12;
#pragma unroll
  for (int j = 0; j < 3; ++j) c3o[j] = (j + rot2) % 3;
#define WOFF(s) ((((s) / 36) * 36 + kso[((s) % 36) / 3] * 3 + c3o[(s) % 3]) * 1024)
  bf16x8 w[K1_D];
#pragma unroll
  for (int s = 0; s < K1_D; ++s) w[s] = ld16(wp + WOFF(s));
  SB0();
  {
    if (!ok) { xr[0] = bf16x8{}; xr[1] = bf16x8{}; xr[2] = bf16x8{}; }
    bf16x8 h[3]; ln_row(xr, h);
#pragma unroll
    for (int i = 0; i < 3; ++i) *(bf16x8*)(Ht + slot * ROWB + 16 * ((r16 + 16 * i) ^ swz(slot & 15))) = h[i];
    asm volatile("" : "+v"(xr[0]), "+v"(xr[1]), "+v"(xr[2]));
  }
  lds_barrier();
  f32x4 acc[3][2];
#define HFRAG(u, jk) (*(const bf16x8*)(Ht + (16 * (u) + r16) * ROWB + 16 * ((4 * kso[jk] + g) ^ swz(r16))))
#define K1_GEMM(MAT, TR) do {                                                              \
    bf16x8 hb[2][2];                                                                        \
    hb[0][0] = HFRAG(0, 0); hb[0][1] = HFRAG(1, 0);                                         \
    _Pragma("unroll") for (int c = 0; c < 3; ++c) { acc[c][0] = f32x4{}; acc[c][1] = f32x4{}; } \
    _Pragma("unroll") for (int jk = 0; jk < 12; ++jk) {                                     \
      if (jk < 11) { hb[(jk + 1) & 1][0] = HFRAG(0, jk + 1); hb[(jk + 1) & 1][1] = HFRAG(1, jk + 1); } \
      _Pragma("unroll") for (int c = 0; c < 3; ++c) {                                       \
        const int s = (MAT) * 36 + jk * 3 + c;                                              \
        const bf16x8 wf = w[s % K1_D];                                                      \
        if (s + K1_D < 108) w[s % K1_D] = ld16(wp + WOFF(s + K1_D));                        \
        if (TR) { acc[c][0] = MFMA16(wf, hb[jk & 1][0], acc[c][0]); acc[c][1] = MFMA16(wf, hb[jk & 1][1], acc[c][1]); } \
        else    { acc[c][0] = MFMA16(hb[jk & 1][0], wf, acc[c][0]); acc[c][1] = MFMA16(hb[jk & 1][1], wf, acc[c][1]); } \
        SB0();                                                                              \
      }                                                                                     \
    }                                                                                       \
  } while (0)

  K1_GEMM(0, true);
#pragma unroll
  for (int u = 0; u < 2; ++u) {
    bf16x4 hs[3];
#pragma unroll
    for (int c = 0; c < 3; ++c)
      hs[c] = *(const bf16x4*)(Ht + (16 * u + r16) * ROWB + 8 * (g & 1) + 16 * ((6 * wid + 2 * c3o[c] + (g >> 1)) ^ swz(r16)));
    const bf16x4 z = {};
    f32x4 sacc = MFMA16(cat8(hs[0], hs[1]), cat8(pack4(acc[0][u]), pack4(acc[1][u])), f32x4{});
    sacc = MFMA16(cat8(hs[2], z), cat8(pack4(acc[2][u]), z), sacc);
    *(f32x4*)(Sp + (wid * 2 + u) * 1024 + lane * 16) = sacc;
  }
  SB0();
  K1_GEMM(1, false);
  lds_barrier();
  {
    bf16x8 pf[2];
    const bool blk = (g >> 1) == (r16 >> 3);
#pragma unroll
    for (int u = 0; u < 2; ++u) {
      f32x4 sp[8];
#pragma unroll
      for (int ww = 0; ww < 8; ++ww) sp[ww] = *(const f32x4*)(Sp + (ww * 2 + u) * 1024 + lane * 16);
      f32x4 s = ((sp[0] + sp[1]) + (sp[2] + sp[3])) + ((sp[4] + sp[5]) + (sp[6] + sp[7]));
      if (g & 1) s[3] = NEG;
      const float mx = xmax16(fmaxf(fmaxf(s[0], s[1]), fmaxf(s[2], s[3])));
      f32x4 p;
#pragma unroll
      for (int r = 0; r < 4; ++r) p[r] = __builtin_amdgcn_exp2f(s[r] - mx);
      const float sum = xsum16((p[0] + p[1]) + (p[2] + p[3]));
      const float inv = blk ? __builtin_amdgcn_rcpf(sum) : 0.f;
      const bf16x4 pb = pack4(p * inv), z = {};
      pf[u] = u == 0 ? cat8(pb, z) : cat8(z, pb);
    }
#pragma unroll
    for (int c = 0; c < 3; ++c) {
      const bf16x8 vf = cat8(pack4(acc[c][0]), pack4(acc[c][1]));
#pragma unroll
      for (int u = 0; u < 2; ++u)
        *(f32x4*)(Mx + (16 * u + r16) * MIXB + (48 * wid + 16 * c3o[c] + 4 * g) * 4) = MFMA16(vf, pf[u], f32x4{});
    }
  }
  lds_barrier();
  {
    bf16x8 x1[3], h[3];
#pragma unroll
    for (int i = 0; i < 3; ++i) {
      f32x4 m0 = *(const f32x4*)(Mx + slot * MIXB + (r16 + 16 * i) * 32), m1 = *(const f32x4*)(Mx + slot * MIXB + (r16 + 16 * i) * 32 + 16);
#pragma unroll
      for (int j = 0; j < 4; ++j) { m0[j] += (float)xr[i][j]; m1[j] += (float)xr[i][4 + j]; }
      x1[i] = cat8(pack4(m0), pack4(m1));
    }
    ln_row(x1, h);
#pragma unroll
    for (int i = 0; i < 3; ++i) *(bf16x8*)(Ht + slot * ROWB + 16 * ((r16 + 16 * i) ^ swz(slot & 15))) = h[i];
    if (ok) {
      char* xo = ws + WS_X1 + (size_t)(t * NN + n) * ROWB + r16 * 16;
      char* ho = ws + WS_H1 + (size_t)(t * NP + n) * ROWB;
#pragma unroll
      for (int i = 0; i < 3; ++i) {
        __builtin_nontemporal_store(x1[i], (bf16x8*)(xo + i * 256));
        __builtin_nontemporal_store(h[i], (bf16x8*)(ho + 16 * ((r16 + 16 * i) ^ swz(n & 15))));
      }
    }
  }
  lds_barrier();
  K1_GEMM(2, true);
#pragma unroll
  for (int u = 0; u < 2; ++u)
#pragma unroll
    for (int c = 0; c < 3; ++c) *(bf16x4*)(Mx + (16 * u + r16) * 800 + (48 * wid + 16 * c3o[c] + 4 * g) * 2) = pack4(acc[c][u]);
  lds_barrier();
  if (ok) {
    char* qo = ws + WS_QS + (size_t)(t * NP + n) * ROWB;
#pragma unroll
    for (int i = 0; i < 3; ++i)
      __builtin_nontemporal_store(*(const bf16x8*)(Mx + slot * 800 + (r16 + 16 * i) * 16), (bf16x8*)(qo + 16 * ((r16 + 16 * i) ^ swz(n & 15))));
  }
}

constexpr int SLOT = 24576;
constexpr int K2_P = 6 * SLOT;
constexpr int K2_MX = K2_P + 8192;
constexpr int K2_L = K2_MX + 512;
constexpr int K2_AL = K2_L + 512;
constexpr int K2_FL = K2_AL + 128;
constexpr int K2_LDS = K2_FL + 64;
constexpr float T_BIG = 1.8446744e19f;

DI void dma6(const char* src, int dst, int vo) {
  asm volatile("s_mov_b32 m0, %3\n\ts_nop 0\n\t"
               "global_load_lds_dwordx4 %0, %2\n\tglobal_load_lds_dwordx4 %0, %2 offset:1024\n\t"
               "global_load_lds_dwordx4 %0, %2 offset:2048\n\tglobal_load_lds_dwordx4 %0, %2 offset:3072\n\t"
               "s_mov_b32 m0, %4\n\ts_nop 0\n\t"
               "global_load_lds_dwordx4 %1, %2\n\tglobal_load_lds_dwordx4 %1, %2 offset:1024"
               :: "v"(vo), "v"(vo + 4096), "s"(src), "s"(dst), "s"(dst + 4096) : "memory");
}
#define VMWAIT(n) asm volatile("s_waitcnt vmcnt(" #n ")" ::: "memory")
DI float vmax(float a, float b) { float r; asm("v_max_f32 %0, %1, %2" : "=v"(r) : "v"(a), "v"(b)); return r; }
DI float vmax3(float a, float b, float c) { float r; asm("v_max3_f32 %0, %1, %2, %3" : "=v"(r) : "v"(a), "v"(b), "v"(c)); return r; }
DI float xm16(float v) { auto r = __builtin_amdgcn_permlane16_swap(__float_as_uint(v), __float_as_uint(v), false, false); return vmax(__uint_as_float(r[0]), __uint_as_float(r[1])); }
DI float xm32(float v) { auto r = __builtin_amdgcn_permlane32_swap(__float_as_uint(v), __float_as_uint(v), false, false); return vmax(__uint_as_float(r[0]), __uint_as_float(r[1])); }
#define LDSU(off) (*(volatile LDS_AS unsigned*)(sm + (off)))

__global__ __launch_bounds__(768, 3) void k2_spatial(const char* __restrict__ ws, char* __restrict__ out) {
  extern __shared__ __attribute__((aligned(16))) char sm[];
  const int f = blockIdx.x & 7, qt = blockIdx.x >> 3;
  if (f >= NT) return;
  const int tid = threadIdx.x, wid = __builtin_amdgcn_readfirstlane(tid >> 6), lane = tid & 63, g = lane >> 4, r16 = lane & 15;
  const int w4 = wid & 3;
  const int rotw = qt % 12;
  const char* wv = ws + WS_WVOS;
  const int dvo = lane * 16 + w4 * 6144, dq = w4 * 6144;
#define WCH(j) (wv + ((rotw + (j)) % 12) * SLOT)

  if (wid >= 8) {
    const char* kv = ws + WS_H1 + (size_t)f * NP * ROWB;
#define KVH(k) (kv + ((qt + (k)) % 23) * SLOT)
#define BAR() do { __builtin_amdgcn_s_barrier(); asm volatile("" ::: "memory"); } while (0)
    dma6(ws + WS_QS + (size_t)(f * NP + 32 * qt) * ROWB, 4 * SLOT + dq, dvo);
    dma6(KVH(0), dq, dvo); dma6(KVH(1), SLOT + dq, dvo);
    dma6(KVH(2), 2 * SLOT + dq, dvo); dma6(KVH(3), 3 * SLOT + dq, dvo);
    if (w4 == 0 && lane < 16) LDSU(K2_FL + 4 * lane) = 0u;
    VMWAIT(12); lds_barrier();
    BAR();
    dma6(KVH(4), 4 * SLOT + dq, dvo);
    VMWAIT(6);
#pragma unroll
    for (int i = 1; i <= 12; ++i) {
      BAR();
      const int fb = ((i + 2) % 3) * 2 * SLOT;
      const unsigned fl = LDSU(K2_FL + 4 * (i - 1));
      if (i == 1) { dma6(KVH(5), 5 * SLOT + dq, dvo); dma6(KVH(6), dq, dvo); dma6(KVH(7), SLOT + dq, dvo); }
      if (i >= 2) {
        dma6(i <= 9 ? KVH(2 * i + 4) : WCH(2 * i - 20), fb + dq, dvo);
        if (i != 9 && i != 12) dma6(i <= 8 ? KVH(2 * i + 5) : WCH(2 * i - 19), fb + SLOT + dq, dvo);
        if (__builtin_amdgcn_readfirstlane(fl)) { BAR(); BAR(); }
      }
      if (i == 9) VMWAIT(6); else if (i < 12) VMWAIT(12);
    }
    BAR();
    VMWAIT(18); BAR();
    VMWAIT(6); BAR();
    dma6(WCH(5), 5 * SLOT + dq, dvo); dma6(WCH(6), dq, dvo); dma6(WCH(7), SLOT + dq, dvo);
    VMWAIT(12); BAR();
    dma6(WCH(8), 2 * SLOT + dq, dvo); dma6(WCH(9), 3 * SLOT + dq, dvo);
    VMWAIT(12); BAR();
    dma6(WCH(10), 4 * SLOT + dq, dvo); dma6(WCH(11), 5 * SLOT + dq, dvo);
    VMWAIT(12); BAR();
    VMWAIT(0); BAR();
    return;
  }
  if (wid < 4) {
    __builtin_amdgcn_s_setprio(2);
    const int fsw = swz(r16);
    int roff[2][4];
#pragma unroll
    for (int b = 0; b < 4; ++b) { roff[0][b] = (16 * w4 + r16) * ROWB + 16 * ((4 * b + g) ^ fsw); roff[1][b] = roff[0][b] + 4 * SLOT; }
#define KFRAG(tbuf, ks) (*(const bf16x8*)(sm + roff[(tbuf) >> 1][(ks) & 3] + ((tbuf) & 1) * 2 * SLOT + ((ks) >> 2) * 256))
    const int pw_off = K2_P + r16 * 128 + 16 * ((2 * w4 + (g >> 1)) ^ (r16 & 7)) + 8 * (g & 1);
    const int kpad = (w4 & 1) ? 22 - qt : -1;
    bf16x8 qf[2][12];
    float R[2], l[2] = {0.f, 0.f}, ls[2];
    f32x4 s[2];
    auto mask_u = [&](int i, int u) {
      const int kidx = 2 * i + (w4 >> 1);
      if (kidx >= 23 || kidx == kpad) {
#pragma unroll
        for (int r = 0; r < 4; ++r) s[u][r] = (kidx >= 23 || 4 * g + r >= 9) ? NEG : s[u][r];
      }
    };
    auto put_p = [&](int i) {
      f32x4 p[2];
#pragma unroll
      for (int r = 0; r < 4; ++r) { p[0][r] = __builtin_amdgcn_exp2f(s[0][r] - R[0]); p[1][r] = __builtin_amdgcn_exp2f(s[1][r] - R[1]); }
      *(bf16x4*)(sm + (i & 1) * 4096 + pw_off) = pack4(p[0]); *(bf16x4*)(sm + (i & 1) * 4096 + 2048 + pw_off) = pack4(p[1]);
      ls[0] = (p[0][0] + p[0][1]) + (p[0][2] + p[0][3]); ls[1] = (p[1][0] + p[1][1]) + (p[1][2] + p[1][3]);
    };
    auto put_max = [&]() {
      const float m0 = xm32(xm16(vmax(vmax3(s[0][0], s[0][1], s[0][2]), s[0][3]))), m1 = xm32(xm16(vmax(vmax3(s[1][0], s[1][1], s[1][2]), s[1][3])));
      if (g == 0) { *(float*)(sm + K2_MX + r16 * 16 + w4 * 4) = m0; *(float*)(sm + K2_MX + (16 + r16) * 16 + w4 * 4) = m1; }
    };
    auto get_max = [&](float (&M)[2]) {
#pragma unroll
      for (int u = 0; u < 2; ++u) {
        const f32x4 mm = *(const f32x4*)(sm + K2_MX + (16 * u + r16) * 16);
        M[u] = vmax(vmax3(mm[0], mm[1], mm[2]), mm[3]);
      }
    };
    auto redo = [&](int i) {
      put_max();
      lds_barrier();
      float M[2], a[2]; get_max(M);
#pragma unroll
      for (int u = 0; u < 2; ++u) { const float rn = vmax(R[u], M[u]); a[u] = __builtin_amdgcn_exp2f(R[u] - rn); R[u] = rn; l[u] *= a[u]; }
      put_p(i - 1);
      if (wid == 0 && g == 0) { *(float*)(sm + K2_AL + r16 * 4) = a[0]; *(float*)(sm + K2_AL + 64 + r16 * 4) = a[1]; }
      lds_barrier();
    };

    lds_barrier();
    {
      bf16x8 kf[12];
#pragma unroll
      for (int ks = 0; ks < 12; ++ks) {
        kf[ks] = *(const bf16x8*)(sm + roff[0][ks & 3] + (ks >> 2) * 256);
        qf[0][ks] = *(const bf16x8*)(sm + roff[1][ks & 3] - 16 * w4 * ROWB + (ks >> 2) * 256);
        qf[1][ks] = *(const bf16x8*)(sm + roff[1][ks & 3] - 16 * w4 * ROWB + 16 * ROWB + (ks >> 2) * 256);
      }
      s[0] = f32x4{}; s[1] = f32x4{};
#pragma unroll
      for (int ks = 0; ks < 12; ++ks) { s[0] = MFMA16(kf[ks], qf[0][ks], s[0]); s[1] = MFMA16(kf[ks], qf[1][ks], s[1]); }
      mask_u(0, 0); mask_u(0, 1);
      put_max();
    }
    lds_barrier();
    get_max(R);
    put_p(0);
#pragma unroll
    for (int i = 1; i <= 11; ++i) {
      lds_barrier();
      bf16x8 kf[12];
      const unsigned fl = LDSU(K2_FL + 4 * (i - 1));
#pragma unroll
      for (int ks = 0; ks < 6; ++ks) kf[ks] = KFRAG(i % 3, ks);
      if (i > 1) { if (__builtin_amdgcn_readfirstlane(fl)) redo(i); }
      l[0] += ls[0]; l[1] += ls[1];
      SB0();
      s[0] = f32x4{}; s[1] = f32x4{};
#pragma unroll
      for (int ks = 0; ks < 12; ++ks) {
        s[0] = MFMA16(kf[ks], qf[0][ks], s[0]);
        s[1] = MFMA16(kf[ks], qf[1][ks], s[1]);
        if (ks < 6) kf[6 + ks] = KFRAG(i % 3, 6 + ks);
        SB0();
      }
      mask_u(i, 0); mask_u(i, 1);
      put_p(i);
      if (__any(!(ls[0] + ls[1] < T_BIG))) { if (lane == 0) LDSU(K2_FL + 4 * i) = 1u; }
    }
    lds_barrier();
    if (__builtin_amdgcn_readfirstlane(LDSU(K2_FL + 4 * 11))) redo(12);
#pragma unroll
    for (int u = 0; u < 2; ++u) {
      const float t = xsum32(xsum16(l[u] + ls[u]));
      if (g == 0) *(float*)(sm + K2_L + (16 * u + r16) * 16 + w4 * 4) = t;
    }
    lds_barrier();
    __builtin_amdgcn_s_setprio(0);
  } else {
    const int vsw = (8 * (g & 1)) | ((r16 >> 2) << 1);
    int voff[2][6];
#pragma unroll
    for (int i = 0; i < 6; ++i) {
      voff[0][i] = (8 * g + (r16 >> 2)) * ROWB + 16 * ((12 * w4 + 2 * i + ((lane & 3) >> 1)) ^ vsw) + 8 * (lane & 1);
      voff[1][i] = voff[0][i] + 3 * SLOT;
    }
    int pr_off[2];
#pragma unroll
    for (int hh = 0; hh < 2; ++hh) pr_off[hh] = K2_P + r16 * 128 + 16 * ((4 * hh + g) ^ (r16 & 7));
    f32x4 o[6][2];
#pragma unroll
    for (int i = 0; i < 6; ++i) { o[i][0] = f32x4{}; o[i][1] = f32x4{}; }
    bf16x8 vf[2][6];
    auto ld_v = [&](int tbuf, int hh, int c) {
      const int sl = 2 * tbuf + hh;
      return cat8(tr_read(sm + voff[sl / 3][c] + (sl % 3) * SLOT), tr_read(sm + voff[sl / 3][c] + (sl % 3) * SLOT + 4 * ROWB));
    };
    auto redo_wait = [&]() {
      lds_barrier(); lds_barrier();
      const float a0 = *(const float*)(sm + K2_AL + r16 * 4), a1 = *(const float*)(sm + K2_AL + 64 + r16 * 4);
#pragma unroll
      for (int c = 0; c < 6; ++c) { o[c][0] *= a0; o[c][1] *= a1; }
    };
    lds_barrier();
#pragma unroll
    for (int c = 0; c < 6; ++c) { vf[0][c] = ld_v(0, 0, c); vf[1][c] = ld_v(0, 1, c); }
    lds_barrier();
#pragma unroll
    for (int i = 1; i <= 11; ++i) {
      lds_barrier();
      const int sp = ((i - 1) & 1) * 4096;
      const unsigned fl = LDSU(K2_FL + 4 * (i - 1));
      bf16x8 pf[2][2];
#pragma unroll
      for (int hh = 0; hh < 2; ++hh) { pf[hh][0] = *(const bf16x8*)(sm + sp + pr_off[hh]); pf[hh][1] = *(const bf16x8*)(sm + sp + pr_off[hh] + 2048); }
      if (i > 1) {
        if (__builtin_amdgcn_readfirstlane(fl)) {
          redo_wait();
#pragma unroll
          for (int hh = 0; hh < 2; ++hh) { pf[hh][0] = *(const bf16x8*)(sm + sp + pr_off[hh]); pf[hh][1] = *(const bf16x8*)(sm + sp + pr_off[hh] + 2048); }
        }
      }
      SB0();
#pragma unroll
      for (int hh = 0; hh < 2; ++hh)
#pragma unroll
        for (int c = 0; c < 6; ++c) {
          o[c][0] = MFMA16(vf[hh][c], pf[hh][0], o[c][0]);
          o[c][1] = MFMA16(vf[hh][c], pf[hh][1], o[c][1]);
          if (i < 11 || hh == 0) vf[hh][c] = ld_v(i % 3, hh, c);
          SB0();
        }
    }
    lds_barrier();
    if (__builtin_amdgcn_readfirstlane(LDSU(K2_FL + 4 * 11))) redo_wait();
    {
      const bf16x8 p0 = *(const bf16x8*)(sm + 4096 + pr_off[0]), p1 = *(const bf16x8*)(sm + 4096 + pr_off[0] + 2048);
#pragma unroll
      for (int c = 0; c < 6; ++c) { o[c][0] = MFMA16(vf[0][c], p0, o[c][0]); o[c][1] = MFMA16(vf[0][c], p1, o[c][1]); }
    }
    lds_barrier();
    int lanee = __builtin_amdgcn_mbcnt_hi(-1, __builtin_amdgcn_mbcnt_lo(-1, 0));
    asm volatile("" : "+v"(lanee));
    const int ge = lanee >> 4, r16e = lanee & 15;
#pragma unroll
    for (int u = 0; u < 2; ++u) {
      const f32x4 ll = *(const f32x4*)(sm + K2_L + (16 * u + r16e) * 16);
      const float inv = __builtin_amdgcn_rcpf((ll[0] + ll[1]) + (ll[2] + ll[3]));
#pragma unroll
      for (int c = 0; c < 6; ++c)
        *(bf16x4*)(sm + 5 * SLOT + (16 * u + r16e) * ROWB + 16 * ((12 * w4 + 2 * c + (ge >> 1)) ^ swz(r16e)) + 8 * (ge & 1)) = pack4(o[c][u] * inv);
    }
  }

  int lanet = __builtin_amdgcn_mbcnt_hi(-1, __builtin_amdgcn_mbcnt_lo(-1, 0));
  asm volatile("" : "+v"(lanet));
  const int gt = lanet >> 4, r16t = lanet & 15;
  bf16x4 xres[3][2];
#pragma unroll
  for (int u = 0; u < 2; ++u) {
    const int q = 32 * qt + 16 * u + r16t;
    const char* xp = ws + WS_X1 + (size_t)(f * NN + (q < NN ? q : 0)) * ROWB + (48 * wid + 4 * gt) * 2;
#pragma unroll
    for (int c = 0; c < 3; ++c) xres[c][u] = *(const bf16x4*)(xp + c * 32);
  }
  lds_barrier();
  bf16x8 of[2][12];
#pragma unroll
  for (int j = 0; j < 12; ++j)
#pragma unroll
    for (int u = 0; u < 2; ++u) of[u][j] = *(const bf16x8*)(sm + 5 * SLOT + (16 * u + r16t) * ROWB + 16 * ((4 * ((rotw + j) % 12) + gt) ^ swz(r16t)));
  f32x4 acc[3][2] = {};
#pragma unroll
  for (int e = 0; e < 6; ++e) {
    if (e > 0) lds_barrier();
    bf16x8 wf[6];
#pragma unroll
    for (int k2 = 0; k2 < 2; ++k2)
#pragma unroll
      for (int c = 0; c < 3; ++c) wf[k2 * 3 + c] = *(const bf16x8*)(sm + ((2 * e + k2) % 6) * SLOT + (3 * wid + c) * 1024 + lanet * 16);
#pragma unroll
    for (int k2 = 0; k2 < 2; ++k2)
#pragma unroll
      for (int c = 0; c < 3; ++c) {
        acc[c][0] = MFMA16(wf[k2 * 3 + c], of[0][2 * e + k2], acc[c][0]);
        acc[c][1] = MFMA16(wf[k2 * 3 + c], of[1][2 * e + k2], acc[c][1]);
      }
  }
#pragma unroll
  for (int u = 0; u < 2; ++u) {
    const int q = 32 * qt + 16 * u + r16t;
    if (q < NN) {
      const size_t off = (size_t)(f * NN + q) * ROWB + (48 * wid + 4 * gt) * 2;
#pragma unroll
      for (int c = 0; c < 3; ++c) {
        f32x4 v = acc[c][u];
#pragma unroll
        for (int r = 0; r < 4; ++r) v[r] += (float)xres[c][u][r];
        *(bf16x4*)(out + off + c * 32) = pack4(v);
      }
    }
  }
}

}

extern "C" void kernel_launch(void* const* d_in, const int* in_sizes, int n_in,
                              void* d_out, int out_size, void* d_ws, size_t ws_size,
                              hipStream_t stream) {
  (void)in_sizes; (void)n_in; (void)out_size; (void)ws_size;
  const char* x = (const char*)d_in[0];
  char* ws = (char*)d_ws;
  hipFuncSetAttribute((const void*)k2_spatial, hipFuncAttributeMaxDynamicSharedMemorySize, K2_LDS);
  k0_fold<<<256, 256, 0, stream>>>((const char*)d_in[1], (const char*)d_in[2], (const char*)d_in[3], (const char*)d_in[4], ws);
  k1_temporal<<<183, 512, 0, stream>>>(x, ws);
  k2_spatial<<<184, 768, K2_LDS, stream>>>(ws, (char*)d_out);
}
```

```cpp
#include <hip/hip_runtime.h>
#include <hip/hip_bf16.h>
#include <stdint.h>

typedef __attribute__((ext_vector_type(8))) __bf16 bf16x8;
typedef __attribute__((ext_vector_type(4))) __bf16 bf16x4;
typedef __attribute__((ext_vector_type(4))) float f32x4;
typedef __attribute__((ext_vector_type(4))) unsigned u32x4;
typedef __attribute__((ext_vector_type(2))) unsigned u32x2;
#define LDS_AS __attribute__((address_space(3)))
#define GLB_AS __attribute__((address_space(1)))
#define DI __device__ __forceinline__
#define MFMA16(a, b, c) __builtin_amdgcn_mfma_f32_16x16x32_bf16((a), (b), (c), 0, 0, 0)

namespace {

constexpr int NT = 7, NN = 729, NP = 736;
constexpr int ROWB = 768;
constexpr size_t WS_W1   = 0;
constexpr size_t WS_WVOS = 884736;
constexpr size_t WS_X1   = WS_WVOS + 294912;
constexpr size_t WS_H1   = WS_X1 + 3919872;
constexpr size_t WS_QS   = WS_H1 + 3956736;
constexpr float QSCALE = 0.05103103630798288f * 1.4426950408889634f;
constexpr float NEG = -1e30f;

DI int swz(int k) { return (k & 8) | ((k & 3) << 1); }
DI void lds_barrier() { asm volatile("s_waitcnt lgkmcnt(0)" ::: "memory"); __builtin_amdgcn_s_barrier(); asm volatile("" ::: "memory"); }
DI bf16x8 ld16(const void* p) { return *(const bf16x8*)p; }
DI bf16x4 pack4(f32x4 v) { bf16x4 r = {(__bf16)v[0], (__bf16)v[1], (__bf16)v[2], (__bf16)v[3]}; return r; }
DI bf16x8 cat8(bf16x4 a, bf16x4 b) { return __builtin_shufflevector(a, b, 0, 1, 2, 3, 4, 5, 6, 7); }
DI bf16x4 tr_read(const char* p) { return __builtin_amdgcn_ds_read_tr16_b64_v4bf16((LDS_AS bf16x4*)p); }
template <int CTRL> DI float dpp_mov(float v) { return __int_as_float(__builtin_amdgcn_update_dpp(0, __float_as_int(v), CTRL, 0xf, 0xf, false)); }
DI float red16_sum(float v) {
  v += dpp_mov<0x128>(v); v += dpp_mov<0x124>(v); v += dpp_mov<0x122>(v); v += dpp_mov<0x121>(v); return v;
}
DI float xmax16(float v) { auto r = __builtin_amdgcn_permlane16_swap(__float_as_uint(v), __float_as_uint(v), false, false); return fmaxf(__uint_as_float(r[0]), __uint_as_float(r[1])); }
DI float xmax32(float v) { auto r = __builtin_amdgcn_permlane32_swap(__float_as_uint(v), __float_as_uint(v), false, false); return fmaxf(__uint_as_float(r[0]), __uint_as_float(r[1])); }
DI float xsum16(float v) { auto r = __builtin_amdgcn_permlane16_swap(__float_as_uint(v), __float_as_uint(v), false, false); return __uint_as_float(r[0]) + __uint_as_float(r[1]); }
DI float xsum32(float v) { auto r = __builtin_amdgcn_permlane32_swap(__float_as_uint(v), __float_as_uint(v), false, false); return __uint_as_float(r[0]) + __uint_as_float(r[1]); }
#define SB0() __builtin_amdgcn_sched_barrier(0)

DI void ln_row(const bf16x8 (&xin)[3], bf16x8 (&hout)[3]) {
  f32x4 v[6];
#pragma unroll
  for (int i = 0; i < 3; ++i)
#pragma unroll
    for (int j = 0; j < 4; ++j) { v[2 * i][j] = (float)xin[i][j]; v[2 * i + 1][j] = (float)xin[i][4 + j]; }
  const f32x4 s4 = ((v[0] + v[1]) + (v[2] + v[3])) + (v[4] + v[5]);
  const float mean = red16_sum((s4[0] + s4[1]) + (s4[2] + s4[3])) * (1.f / 384.f);
  f32x4 q4 = {};
#pragma unroll
  for (int k = 0; k < 6; ++k) { v[k] -= mean; q4 += v[k] * v[k]; }
  const float rstd = rsqrtf(red16_sum((q4[0] + q4[1]) + (q4[2] + q4[3])) * (1.f / 384.f) + 1e-5f);
#pragma unroll
  for (int i = 0; i < 3; ++i) hout[i] = cat8(pack4(v[2 * i] * rstd), pack4(v[2 * i + 1] * rstd));
}

__global__ __launch_bounds__(256) void k0_fold(const char* __restrict__ wqkv_t, const char* __restrict__ wo_t,
                                               const char* __restrict__ wqkv_s, const char* __restrict__ wo_s,
                                               char* __restrict__ ws) {
  __shared__ __attribute__((aligned(16))) char sm[384 * 112];
  const int b = blockIdx.x, mat = (b & 7) >> 1, ti = 4 * (b & 1) + (b >> 6), tj = (b >> 3) & 7;
  const int tid = threadIdx.x, wid = __builtin_amdgcn_readfirstlane(tid >> 6), lane = tid & 63, g = lane >> 4, r16 = lane & 15;
  const char* wqkv = mat < 2 ? wqkv_t : wqkv_s;
  const char* wo = mat < 2 ? wo_t : wo_s;
  const bool isqk = (mat & 1) == 0;
  bf16x8 a[12];
  if (wid < 3) {
    const char* ap = wqkv + ((48 * ti + 16 * wid + r16) * 1152 + (isqk ? 0 : 768) + 8 * g) * 2;
#pragma unroll
    for (int ks = 0; ks < 12; ++ks) a[ks] = ld16(ap + ks * 64);
  }
  if (isqk) {
#pragma unroll
    for (int it = 0; it < 9; ++it) {
      const int idx = tid + 256 * it, row = idx / 48, ch = idx % 48;
      *(bf16x8*)(sm + row * 768 + 16 * (ch ^ swz(row & 15))) = ld16(wqkv + ((48 * tj + row) * 1152 + 384 + ch * 8) * 2);
    }
  } else {
#pragma unroll
    for (int it = 0; it < 9; ++it) {
      const int idx = tid + 256 * it, row = idx / 6, ch = idx % 6;
      *(bf16x8*)(sm + row * 112 + ch * 16) = ld16(wo + (row * 384 + 48 * tj + ch * 8) * 2);
    }
  }
  if (wid == 3 && b < 14) {
    char* dst = ws + (b < 7 ? WS_H1 : WS_QS) + ((size_t)(b % 7) * NP + NN) * ROWB;
    const u32x4 z = {0u, 0u, 0u, 0u};
    for (int i = lane; i < 7 * 48; i += 64) *(u32x4*)(dst + i * 16) = z;
  }
  __syncthreads();
  if (wid < 3) {
    f32x4 acc[3] = {};
    if (isqk) {
#pragma unroll
      for (int ks = 0; ks < 12; ++ks)
#pragma unroll
        for (int jt = 0; jt < 3; ++jt)
          acc[jt] = MFMA16(a[ks], *(const bf16x8*)(sm + (16 * jt + r16) * 768 + 16 * ((4 * ks + g) ^ swz(r16))), acc[jt]);
    } else {
      const char* tb = sm + (8 * g + (r16 >> 2)) * 112 + (lane & 3) * 8;
#pragma unroll
      for (int ks = 0; ks < 12; ++ks)
#pragma unroll
        for (int jt = 0; jt < 3; ++jt) {
          const bf16x4 lo = tr_read(tb + (32 * ks) * 112 + jt * 32), hi = tr_read(tb + (32 * ks + 4) * 112 + jt * 32);
          acc[jt] = MFMA16(a[ks], cat8(lo, hi), acc[jt]);
        }
    }
    const float sc = isqk ? QSCALE : 1.f;
    const int IT = 3 * ti + wid, ks = IT >> 1, gp = 2 * (IT & 1) + (g >> 1);
#pragma unroll
    for (int jt = 0; jt < 3; ++jt) {
      const int JT = 3 * tj + jt;
      size_t off;
      if (mat == 3) off = WS_WVOS + (size_t)(JT * 12 + ks) * 1024;
      else off = WS_W1 + (size_t)(tj * 108 + mat * 36 + ks * 3 + jt) * 1024;
      *(bf16x4*)(ws + off + (r16 + 16 * gp) * 16 + 8 * (g & 1)) = pack4(acc[jt] * sc);
    }
  }
}

constexpr int K1_D = 12;
constexpr int MIXB = 1552;
__global__ __launch_bounds__(512, 2) void k1_temporal(const char* __restrict__ x, char* __restrict__ ws) {
  __shared__ __attribute__((aligned(16))) char sm[24576 + 32 * MIXB + 16384];
  char* const Ht = sm; char* const Mx = sm + 24576; char* const Sp = sm + 24576 + 32 * MIXB;
  const int tid = threadIdx.x, wid = __builtin_amdgcn_readfirstlane(tid >> 6), lane = tid & 63, g = lane >> 4, r16 = lane & 15;
  const int n0 = blockIdx.x * 4, n = n0 + (wid >> 1);
  const int slot = 4 * wid + g, t = slot & 7;
  const bool ok = (n < NN) && (t < NT);
  bf16x8 xr[3];
  {
    const char* xp = x + ((size_t)(ok ? t * NN + n : 0)) * ROWB + r16 * 16;
#pragma unroll
    for (int i = 0; i < 3; ++i) xr[i] = ld16(xp + i * 256);
  }
  SB0();
  const char* wp = ws + WS_W1 + (size_t)wid * 108 * 1024 + lane * 16;
  const int rot = (blockIdx.x >> 3) % 12, rot2 = ((blockIdx.x >> 3) / 12 + blockIdx.x) % 3;
  int kso[12], c3o[3];
#pragma unroll
  for (int j = 0; j < 12; ++j) kso[j] = (j + rot) % 12;
#pragma unroll
  for (int j = 0; j < 3; ++j) c3o[j] = (j + rot2) % 3;
#define WOFF(s) ((((s) / 36) * 36 + kso[((s) % 36) / 3] * 3 + c3o[(s) % 3]) * 1024)
  bf16x8 w[K1_D];
#pragma unroll
  for (int s = 0; s < 3; ++s) w[s] = ld16(wp + WOFF(s));
  SB0();
  {
    if (!ok) { xr[0] = bf16x8{}; xr[1] = bf16x8{}; xr[2] = bf16x8{}; }
    bf16x8 h[3]; ln_row(xr, h);
#pragma unroll
    for (int i = 0; i < 3; ++i) *(bf16x8*)(Ht + slot * ROWB + 16 * ((r16 + 16 * i) ^ swz(slot & 15))) = h[i];
    asm volatile("" : "+v"(xr[0]), "+v"(xr[1]), "+v"(xr[2]));
  }
  SB0();
#pragma unroll
  for (int s = 3; s < K1_D; ++s) w[s] = ld16(wp + WOFF(s));
  lds_barrier();
  f32x4 acc[3][2];
#define HFRAG(u, jk) (*(const bf16x8*)(Ht + (16 * (u) + r16) * ROWB + 16 * ((4 * kso[jk] + g) ^ swz(r16))))
#define K1_GEMM(MAT, TR, HOOK) do {                                                        \
    bf16x8 hb[2][2];                                                                        \
    hb[0][0] = HFRAG(0, 0); hb[0][1] = HFRAG(1, 0);                                         \
    _Pragma("unroll") for (int c = 0; c < 3; ++c) { acc[c][0] = f32x4{}; acc[c][1] = f32x4{}; } \
    _Pragma("unroll") for (int jk = 0; jk < 12; ++jk) {                                     \
      if (jk < 11) { hb[(jk + 1) & 1][0] = HFRAG(0, jk + 1); hb[(jk + 1) & 1][1] = HFRAG(1, jk + 1); } \
      HOOK;                                                                                 \
      _Pragma("unroll") for (int c = 0; c < 3; ++c) {                                       \
        const int s = (MAT) * 36 + jk * 3 + c;                                              \
        const bf16x8 wf = w[s % K1_D];                                                      \
        if (s + K1_D < 108) w[s % K1_D] = ld16(wp + WOFF(s + K1_D));                        \
        if (TR) { acc[c][0] = MFMA16(wf, hb[jk & 1][0], acc[c][0]); acc[c][1] = MFMA16(wf, hb[jk & 1][1], acc[c][1]); } \
        else    { acc[c][0] = MFMA16(hb[jk & 1][0], wf, acc[c][0]); acc[c][1] = MFMA16(hb[jk & 1][1], wf, acc[c][1]); } \
        SB0();                                                                              \
      }                                                                                     \
    }                                                                                       \
  } while (0)

  K1_GEMM(0, true, );
#pragma unroll
  for (int u = 0; u < 2; ++u) {
    bf16x4 hs[3];
#pragma unroll
    for (int c = 0; c < 3; ++c)
      hs[c] = *(const bf16x4*)(Ht + (16 * u + r16) * ROWB + 8 * (g & 1) + 16 * ((6 * wid + 2 * c3o[c] + (g >> 1)) ^ swz(r16)));
    const bf16x4 z = {};
    f32x4 sacc = MFMA16(cat8(hs[0], hs[1]), cat8(pack4(acc[0][u]), pack4(acc[1][u])), f32x4{});
    sacc = MFMA16(cat8(hs[2], z), cat8(pack4(acc[2][u]), z), sacc);
    *(f32x4*)(Sp + (wid * 2 + u) * 1024 + lane * 16) = sacc;
  }
  SB0();
  bf16x8 pf[2];
  auto tsoftmax = [&](int u) {
    const bool blk = (g >> 1) == (r16 >> 3);
    f32x4 sp[8];
#pragma unroll
    for (int ww = 0; ww < 8; ++ww) sp[ww] = *(const f32x4*)(Sp + (ww * 2 + u) * 1024 + lane * 16);
    f32x4 s = ((sp[0] + sp[1]) + (sp[2] + sp[3])) + ((sp[4] + sp[5]) + (sp[6] + sp[7]));
    if (g & 1) s[3] = NEG;
    const float mx = xmax16(fmaxf(fmaxf(s[0], s[1]), fmaxf(s[2], s[3])));
    f32x4 p;
#pragma unroll
    for (int r = 0; r < 4; ++r) p[r] = __builtin_amdgcn_exp2f(s[r] - mx);
    const float sum = xsum16((p[0] + p[1]) + (p[2] + p[3]));
    const float inv = blk ? __builtin_amdgcn_rcpf(sum) : 0.f;
    const bf16x4 pb = pack4(p * inv), z = {};
    pf[u] = u == 0 ? cat8(pb, z) : cat8(z, pb);
  };
  K1_GEMM(1, false, { if (jk == 5) lds_barrier(); if (jk == 6) tsoftmax(0); if (jk == 8) tsoftmax(1); });
  {
#pragma unroll
    for (int c = 0; c < 3; ++c) {
      const bf16x8 vf = cat8(pack4(acc[c][0]), pack4(acc[c][1]));
#pragma unroll
      for (int u = 0; u < 2; ++u)
        *(f32x4*)(Mx + (16 * u + r16) * MIXB + (48 * wid + 16 * c3o[c] + 4 * g) * 4) = MFMA16(vf, pf[u], f32x4{});
    }
  }
  lds_barrier();
  {
    bf16x8 x1[3], h[3];
#pragma unroll
    for (int i = 0; i < 3; ++i) {
      f32x4 m0 = *(const f32x4*)(Mx + slot * MIXB + (r16 + 16 * i) * 32), m1 = *(const f32x4*)(Mx + slot * MIXB + (r16 + 16 * i) * 32 + 16);
#pragma unroll
      for (int j = 0; j < 4; ++j) { m0[j] += (float)xr[i][j]; m1[j] += (float)xr[i][4 + j]; }
      x1[i] = cat8(pack4(m0), pack4(m1));
    }
    ln_row(x1, h);
#pragma unroll
    for (int i = 0; i < 3; ++i) *(bf16x8*)(Ht + slot * ROWB + 16 * ((r16 + 16 * i) ^ swz(slot & 15))) = h[i];
    if (ok) {
      char* xo = ws + WS_X1 + (size_t)(t * NN + n) * ROWB + r16 * 16;
      char* ho = ws + WS_H1 + (size_t)(t * NP + n) * ROWB;
#pragma unroll
      for (int i = 0; i < 3; ++i) {
        __builtin_nontemporal_store(x1[i], (bf16x8*)(xo + i * 256));
        __builtin_nontemporal_store(h[i], (bf16x8*)(ho + 16 * ((r16 + 16 * i) ^ swz(n & 15))));
      }
    }
  }
  lds_barrier();
  K1_GEMM(2, true, );
#pragma unroll
  for (int u = 0; u < 2; ++u)
#pragma unroll
    for (int c = 0; c < 3; ++c) *(bf16x4*)(Mx + (16 * u + r16) * 800 + (48 * wid + 16 * c3o[c] + 4 * g) * 2) = pack4(acc[c][u]);
  lds_barrier();
  if (ok) {
    char* qo = ws + WS_QS + (size_t)(t * NP + n) * ROWB;
#pragma unroll
    for (int i = 0; i < 3; ++i)
      __builtin_nontemporal_store(*(const bf16x8*)(Mx + slot * 800 + (r16 + 16 * i) * 16), (bf16x8*)(qo + 16 * ((r16 + 16 * i) ^ swz(n & 15))));
  }
}

constexpr int SLOT = 24576;
constexpr int K2_P = 6 * SLOT;
constexpr int K2_MX = K2_P + 8192;
constexpr int K2_L = K2_MX + 512;
constexpr int K2_AL = K2_L + 512;
constexpr int K2_FL = K2_AL + 128;
constexpr int K2_LDS = K2_FL + 64;
constexpr float T_BIG = 1.8446744e19f;

DI void dma6(const char* src, int dst, int vo) {
  asm volatile("s_mov_b32 m0, %3\n\ts_nop 0\n\t"
               "global_load_lds_dwordx4 %0, %2\n\tglobal_load_lds_dwordx4 %0, %2 offset:1024\n\t"
               "global_load_lds_dwordx4 %0, %2 offset:2048\n\tglobal_load_lds_dwordx4 %0, %2 offset:3072\n\t"
               "s_mov_b32 m0, %4\n\ts_nop 0\n\t"
               "global_load_lds_dwordx4 %1, %2\n\tglobal_load_lds_dwordx4 %1, %2 offset:1024"
               :: "v"(vo), "v"(vo + 4096), "s"(src), "s"(dst), "s"(dst + 4096) : "memory");
}
#define VMWAIT(n) asm volatile("s_waitcnt vmcnt(" #n ")" ::: "memory")
DI float vmax(float a, float b) { float r; asm("v_max_f32 %0, %1, %2" : "=v"(r) : "v"(a), "v"(b)); return r; }
DI float vmax3(float a, float b, float c) { float r; asm("v_max3_f32 %0, %1, %2, %3" : "=v"(r) : "v"(a), "v"(b), "v"(c)); return r; }
DI float xm16(float v) { auto r = __builtin_amdgcn_permlane16_swap(__float_as_uint(v), __float_as_uint(v), false, false); return vmax(__uint_as_float(r[0]), __uint_as_float(r[1])); }
DI float xm32(float v) { auto r = __builtin_amdgcn_permlane32_swap(__float_as_uint(v), __float_as_uint(v), false, false); return vmax(__uint_as_float(r[0]), __uint_as_float(r[1])); }
#define LDSU(off) (*(volatile LDS_AS unsigned*)(sm + (off)))

__global__ __launch_bounds__(768, 3) void k2_spatial(const char* __restrict__ ws, char* __restrict__ out) {
  extern __shared__ __attribute__((aligned(16))) char sm[];
  const int f = blockIdx.x & 7, qt = blockIdx.x >> 3;
  if (f >= NT) return;
  const int tid = threadIdx.x, wid = __builtin_amdgcn_readfirstlane(tid >> 6), lane = tid & 63, g = lane >> 4, r16 = lane & 15;
  const int w4 = wid & 3;
  const char* wv = ws + WS_WVOS;
  bf16x8 wreg[24];
  const int rotw = qt % 12;
#define WFR(c, j) ld16(wv + wid * SLOT + ((c) * 12 + ((j) + rotw) % 12) * 1024 + lane * 16)
  const int dvo = lane * 16 + w4 * 6144, dq = w4 * 6144;

  if (wid >= 8) {
    const char* kv = ws + WS_H1 + (size_t)f * NP * ROWB;
#define KVH(k) (kv + ((qt + (k)) % 23) * SLOT)
#define BAR() do { __builtin_amdgcn_s_barrier(); asm volatile("" ::: "memory"); } while (0)
    dma6(ws + WS_QS + (size_t)(f * NP + 32 * qt) * ROWB, 4 * SLOT + dq, dvo);
    dma6(KVH(0), dq, dvo); dma6(KVH(1), SLOT + dq, dvo);
    dma6(KVH(2), 2 * SLOT + dq, dvo); dma6(KVH(3), 3 * SLOT + dq, dvo);
    if (w4 == 0 && lane < 16) LDSU(K2_FL + 4 * lane) = 0u;
    VMWAIT(12); lds_barrier();
    BAR();
    dma6(KVH(4), 4 * SLOT + dq, dvo);
    VMWAIT(6);
#pragma unroll
    for (int i = 1; i <= 12; ++i) {
      BAR();
      const int fb = ((i + 2) % 3) * 2 * SLOT;
      const unsigned fl = LDSU(K2_FL + 4 * (i - 1));
      if (i == 1) { dma6(KVH(5), 5 * SLOT + dq, dvo); dma6(KVH(6), dq, dvo); dma6(KVH(7), SLOT + dq, dvo); }
      if (i >= 2 && i <= 11) {
        dma6(i <= 9 ? KVH(2 * i + 4) : wv + (2 * i - 16) * SLOT, fb + dq, dvo);
        if (i != 9) dma6(i <= 8 ? KVH(2 * i + 5) : wv + (2 * i - 15) * SLOT, fb + SLOT + dq, dvo);
      }
      if (i >= 2 && i <= 9) {
#pragma unroll
        for (int j = 3 * (i - 2); j < 3 * (i - 1); ++j) wreg[j] = WFR(j / 12, j % 12);
      }
      if (i >= 2) { if (__builtin_amdgcn_readfirstlane(fl)) { BAR(); BAR(); } }
      if (i == 1 || i == 10) VMWAIT(12); else if (i == 9) VMWAIT(9); else if (i < 9) VMWAIT(15);
    }
    VMWAIT(0);
  } else
  if (wid < 4) {
    __builtin_amdgcn_s_setprio(2);
    const int fsw = swz(r16);
    int roff[2][4];
#pragma unroll
    for (int b = 0; b < 4; ++b) { roff[0][b] = (16 * w4 + r16) * ROWB + 16 * ((4 * b + g) ^ fsw); roff[1][b] = roff[0][b] + 4 * SLOT; }
#define KFRAG(tbuf, ks) (*(const bf16x8*)(sm + roff[(tbuf) >> 1][(ks) & 3] + ((tbuf) & 1) * 2 * SLOT + ((ks) >> 2) * 256))
    const int pw_off = K2_P + r16 * 128 + 16 * ((2 * w4 + (g >> 1)) ^ (r16 & 7)) + 8 * (g & 1);
    const int kpad = (w4 & 1) ? 22 - qt : -1;
    bf16x8 qf[2][12];
    float R[2], l[2] = {0.f, 0.f}, ls[2];
    f32x4 s[2];
    auto mask_u = [&](int i, int u) {
      const int kidx = 2 * i + (w4 >> 1);
      if (kidx >= 23 || kidx == kpad) {
#pragma unroll
        for (int r = 0; r < 4; ++r) s[u][r] = (kidx >= 23 || 4 * g + r >= 9) ? NEG : s[u][r];
      }
    };
    auto put_p = [&](int i) {
      f32x4 p[2];
#pragma unroll
      for (int r = 0; r < 4; ++r) { p[0][r] = __builtin_amdgcn_exp2f(s[0][r] - R[0]); p[1][r] = __builtin_amdgcn_exp2f(s[1][r] - R[1]); }
      *(bf16x4*)(sm + (i & 1) * 4096 + pw_off) = pack4(p[0]); *(bf16x4*)(sm + (i & 1) * 4096 + 2048 + pw_off) = pack4(p[1]);
      ls[0] = (p[0][0] + p[0][1]) + (p[0][2] + p[0][3]); ls[1] = (p[1][0] + p[1][1]) + (p[1][2] + p[1][3]);
    };
    auto put_max = [&]() {
      const float m0 = xm32(xm16(vmax(vmax3(s[0][0], s[0][1], s[0][2]), s[0][3]))), m1 = xm32(xm16(vmax(vmax3(s[1][0], s[1][1], s[1][2]), s[1][3])));
      if (g == 0) { *(float*)(sm + K2_MX + r16 * 16 + w4 * 4) = m0; *(float*)(sm + K2_MX + (16 + r16) * 16 + w4 * 4) = m1; }
    };
    auto get_max = [&](float (&M)[2]) {
#pragma unroll
      for (int u = 0; u < 2; ++u) {
        const f32x4 mm = *(const f32x4*)(sm + K2_MX + (16 * u + r16) * 16);
        M[u] = vmax(vmax3(mm[0], mm[1], mm[2]), mm[3]);
      }
    };
    auto redo = [&](int i) {
      put_max();
      lds_barrier();
      float M[2], a[2]; get_max(M);
#pragma unroll
      for (int u = 0; u < 2; ++u) { const float rn = vmax(R[u], M[u]); a[u] = __builtin_amdgcn_exp2f(R[u] - rn); R[u] = rn; l[u] *= a[u]; }
      put_p(i - 1);
      if (wid == 0 && g == 0) { *(float*)(sm + K2_AL + r16 * 4) = a[0]; *(float*)(sm + K2_AL + 64 + r16 * 4) = a[1]; }
      lds_barrier();
    };

    lds_barrier();
    {
      bf16x8 kf[12];
#pragma unroll
      for (int ks = 0; ks < 12; ++ks) {
        kf[ks] = *(const bf16x8*)(sm + roff[0][ks & 3] + (ks >> 2) * 256);
        qf[0][ks] = *(const bf16x8*)(sm + roff[1][ks & 3] - 16 * w4 * ROWB + (ks >> 2) * 256);
        qf[1][ks] = *(const bf16x8*)(sm + roff[1][ks & 3] - 16 * w4 * ROWB + 16 * ROWB + (ks >> 2) * 256);
      }
      s[0] = f32x4{}; s[1] = f32x4{};
#pragma unroll
      for (int ks = 0; ks < 12; ++ks) { s[0] = MFMA16(kf[ks], qf[0][ks], s[0]); s[1] = MFMA16(kf[ks], qf[1][ks], s[1]); }
      mask_u(0, 0); mask_u(0, 1);
      put_max();
    }
    lds_barrier();
    get_max(R);
    put_p(0);
#pragma unroll
    for (int i = 1; i <= 11; ++i) {
      lds_barrier();
      bf16x8 kf[6];
      const unsigned fl = LDSU(K2_FL + 4 * (i - 1));
#pragma unroll
      for (int ks = 0; ks < 6; ++ks) kf[ks] = KFRAG(i % 3, ks);
      if (i > 1) { if (__builtin_amdgcn_readfirstlane(fl)) redo(i); }
      l[0] += ls[0]; l[1] += ls[1];
      SB0();
      s[0] = f32x4{}; s[1] = f32x4{};
#pragma unroll
      for (int ks = 0; ks < 12; ++ks) {
        s[0] = MFMA16(kf[ks % 6], qf[0][ks], s[0]);
        s[1] = MFMA16(kf[ks % 6], qf[1][ks], s[1]);
        if (ks < 6) kf[ks] = KFRAG(i % 3, 6 + ks);
        SB0();
      }
      mask_u(i, 0); mask_u(i, 1);
      put_p(i);
      if (__any(!(ls[0] + ls[1] < T_BIG))) { if (lane == 0) LDSU(K2_FL + 4 * i) = 1u; }
    }
    lds_barrier();
    if (__builtin_amdgcn_readfirstlane(LDSU(K2_FL + 4 * 11))) redo(12);
#pragma unroll
    for (int u = 0; u < 2; ++u) {
      const float t = xsum32(xsum16(l[u] + ls[u]));
      if (g == 0) *(float*)(sm + K2_L + (16 * u + r16) * 16 + w4 * 4) = t;
    }
    __builtin_amdgcn_s_setprio(0);
#pragma unroll
    for (int j = 0; j < 12; ++j) { wreg[j] = WFR(0, j); wreg[12 + j] = WFR(1, j); }
  } else {
    const int vsw = (8 * (g & 1)) | ((r16 >> 2) << 1);
    int voff[2][6];
#pragma unroll
    for (int i = 0; i < 6; ++i) {
      voff[0][i] = (8 * g + (r16 >> 2)) * ROWB + 16 * ((12 * w4 + 2 * i + ((lane & 3) >> 1)) ^ vsw) + 8 * (lane & 1);
      voff[1][i] = voff[0][i] + 3 * SLOT;
    }
    int pr_off[2];
#pragma unroll
    for (int hh = 0; hh < 2; ++hh) pr_off[hh] = K2_P + r16 * 128 + 16 * ((4 * hh + g) ^ (r16 & 7));
    f32x4 o[6][2];
#pragma unroll
    for (int i = 0; i < 6; ++i) { o[i][0] = f32x4{}; o[i][1] = f32x4{}; }
    bf16x8 vf[2][6];
    auto ld_v = [&](int tbuf, int hh, int c) {
      const int sl = 2 * tbuf + hh;
      return cat8(tr_read(sm + voff[sl / 3][c] + (sl % 3) * SLOT), tr_read(sm + voff[sl / 3][c] + (sl % 3) * SLOT + 4 * ROWB));
    };
    auto redo_wait = [&]() {
      lds_barrier(); lds_barrier();
      const float a0 = *(const float*)(sm + K2_AL + r16 * 4), a1 = *(const float*)(sm + K2_AL + 64 + r16 * 4);
#pragma unroll
      for (int c = 0; c < 6; ++c) { o[c][0] *= a0; o[c][1] *= a1; }
    };
    lds_barrier();
#pragma unroll
    for (int c = 0; c < 6; ++c) { vf[0][c] = ld_v(0, 0, c); vf[1][c] = ld_v(0, 1, c); }
    lds_barrier();
#pragma unroll
    for (int i = 1; i <= 11; ++i) {
      lds_barrier();
      const int sp = ((i - 1) & 1) * 4096;
      const unsigned fl = LDSU(K2_FL + 4 * (i - 1));
      bf16x8 pf[2][2];
#pragma unroll
      for (int hh = 0; hh < 2; ++hh) { pf[hh][0] = *(const bf16x8*)(sm + sp + pr_off[hh]); pf[hh][1] = *(const bf16x8*)(sm + sp + pr_off[hh] + 2048); }
      if (i > 1) {
        if (__builtin_amdgcn_readfirstlane(fl)) {
          redo_wait();
#pragma unroll
          for (int hh = 0; hh < 2; ++hh) { pf[hh][0] = *(const bf16x8*)(sm + sp + pr_off[hh]); pf[hh][1] = *(const bf16x8*)(sm + sp + pr_off[hh] + 2048); }
        }
      }
      SB0();
#pragma unroll
      for (int hh = 0; hh < 2; ++hh)
#pragma unroll
        for (int c = 0; c < 6; ++c) {
          o[c][0] = MFMA16(vf[hh][c], pf[hh][0], o[c][0]);
          o[c][1] = MFMA16(vf[hh][c], pf[hh][1], o[c][1]);
          if (i < 11 || hh == 0) vf[hh][c] = ld_v(i % 3, hh, c);
          SB0();
        }
    }
    lds_barrier();
    if (__builtin_amdgcn_readfirstlane(LDSU(K2_FL + 4 * 11))) redo_wait();
    {
      const bf16x8 p0 = *(const bf16x8*)(sm + 4096 + pr_off[0]), p1 = *(const bf16x8*)(sm + 4096 + pr_off[0] + 2048);
#pragma unroll
      for (int c = 0; c < 6; ++c) { o[c][0] = MFMA16(vf[0][c], p0, o[c][0]); o[c][1] = MFMA16(vf[0][c], p1, o[c][1]); }
    }
    int lanee = __builtin_amdgcn_mbcnt_hi(-1, __builtin_amdgcn_mbcnt_lo(-1, 0));
    asm volatile("" : "+v"(lanee));
    const int ge = lanee >> 4, r16e = lanee & 15;
#pragma unroll
    for (int c = 0; c < 6; ++c) {
      char* op = sm + 5 * SLOT + r16e * ROWB + 16 * ((12 * w4 + 2 * c + (ge >> 1)) ^ swz(r16e)) + 8 * (ge & 1);
      *(bf16x4*)op = pack4(o[c][0]); *(bf16x4*)(op + 16 * ROWB) = pack4(o[c][1]);
    }
  }

  int lanet = __builtin_amdgcn_mbcnt_hi(-1, __builtin_amdgcn_mbcnt_lo(-1, 0));
  asm volatile("" : "+v"(lanet));
  const int gt = lanet >> 4, r16t = lanet & 15;
  bf16x4 xres[2][2];
#pragma unroll
  for (int u = 0; u < 2; ++u) {
    const int q = 32 * qt + 16 * u + r16t;
    const char* xp = ws + WS_X1 + (size_t)(f * NN + (q < NN ? q : 0)) * ROWB + (32 * wid + 4 * gt) * 2;
    xres[0][u] = *(const bf16x4*)xp; xres[1][u] = *(const bf16x4*)(xp + 32);
  }
  int ooff[4];
#pragma unroll
  for (int b = 0; b < 4; ++b) ooff[b] = 5 * SLOT + r16t * ROWB + 16 * ((4 * b + gt) ^ swz(r16t));
  lds_barrier();
  f32x4 acc[2][2] = {};
  float inv[2];
#pragma unroll
  for (int u = 0; u < 2; ++u) {
    const f32x4 ll = *(const f32x4*)(sm + K2_L + (16 * u + r16t) * 16);
    inv[u] = __builtin_amdgcn_rcpf((ll[0] + ll[1]) + (ll[2] + ll[3]));
  }
#define OFRAG(u, ks) (*(const bf16x8*)(sm + ooff[(ks) & 3] + (u) * 16 * ROWB + ((ks) >> 2) * 256))
  if (wid >= 4 && wid < 8) {
    const char* wl = sm + w4 * SLOT + lanet * 16;
#pragma unroll
    for (int ks = 0; ks < 12; ++ks) {
      const bf16x8 o0 = OFRAG(0, ks), o1 = OFRAG(1, ks), w0 = *(const bf16x8*)(wl + ks * 1024), w1 = *(const bf16x8*)(wl + (12 + ks) * 1024);
      acc[0][0] = MFMA16(w0, o0, acc[0][0]); acc[0][1] = MFMA16(w0, o1, acc[0][1]);
      acc[1][0] = MFMA16(w1, o0, acc[1][0]); acc[1][1] = MFMA16(w1, o1, acc[1][1]);
    }
  } else {
#pragma unroll
    for (int ks = 0; ks < 12; ++ks) {
      const char* ob = sm + 5 * SLOT + r16t * ROWB + 16 * ((4 * ((ks + rotw) % 12) + gt) ^ swz(r16t));
      const bf16x8 o0 = *(const bf16x8*)ob, o1 = *(const bf16x8*)(ob + 16 * ROWB);
      acc[0][0] = MFMA16(wreg[ks], o0, acc[0][0]); acc[0][1] = MFMA16(wreg[ks], o1, acc[0][1]);
      acc[1][0] = MFMA16(wreg[12 + ks], o0, acc[1][0]); acc[1][1] = MFMA16(wreg[12 + ks], o1, acc[1][1]);
    }
  }
#pragma unroll
  for (int u = 0; u < 2; ++u) {
    const int q = 32 * qt + 16 * u + r16t;
    if (q < NN) {
      char* op = out + (size_t)(f * NN + q) * ROWB + (32 * wid + 4 * gt) * 2;
#pragma unroll
      for (int c = 0; c < 2; ++c) {
        f32x4 v = acc[c][u];
#pragma unroll
        for (int r = 0; r < 4; ++r) v[r] = v[r] * inv[u] + (float)xres[c][u][r];
        *(bf16x4*)(op + c * 32) = pack4(v);
      }
    }
  }
}

}

extern "C" void kernel_launch(void* const* d_in, const int* in_sizes, int n_in,
                              void* d_out, int out_size, void* d_ws, size_t ws_size,
                              hipStream_t stream) {
  (void)in_sizes; (void)n_in; (void)out_size; (void)ws_size;
  const char* x = (const char*)d_in[0];
  char* ws = (char*)d_ws;
  hipFuncSetAttribute((const void*)k2_spatial, hipFuncAttributeMaxDynamicSharedMemorySize, K2_LDS);
  k0_fold<<<256, 256, 0, stream>>>((const char*)d_in[1], (const char*)d_in[2], (const char*)d_in[3], (const char*)d_in[4], ws);
  k1_temporal<<<183, 512, 0, stream>>>(x, ws);
  k2_spatial<<<184, 768, K2_LDS, stream>>>(ws, (char*)d_out);
}
```

```cpp
#include <hip/hip_runtime.h>
#include <hip/hip_bf16.h>
#include <stdint.h>

typedef __attribute__((ext_vector_type(8))) __bf16 bf16x8;
typedef __attribute__((ext_vector_type(4))) __bf16 bf16x4;
typedef __attribute__((ext_vector_type(4))) float f32x4;
typedef __attribute__((ext_vector_type(4))) unsigned u32x4;
typedef __attribute__((ext_vector_type(2))) unsigned u32x2;
#define LDS_AS __attribute__((address_space(3)))
#define GLB_AS __attribute__((address_space(1)))
#define DI __device__ __forceinline__
#define MFMA16(a, b, c) __builtin_amdgcn_mfma_f32_16x16x32_bf16((a), (b), (c), 0, 0, 0)

namespace {

constexpr int NT = 7, NN = 729, NP = 736;
constexpr int ROWB = 768;
constexpr size_t WS_W1   = 0;
constexpr size_t WS_WVOS = 884736;
constexpr size_t WS_X1   = WS_WVOS + 294912;
constexpr size_t WS_H1   = WS_X1 + 3919872;
constexpr size_t WS_QS   = WS_H1 + 3956736;
constexpr float QSCALE = 0.05103103630798288f * 1.4426950408889634f;
constexpr float NEG = -1e30f;

DI int swz(int k) { return (k & 8) | ((k & 3) << 1); }
DI void lds_barrier() { asm volatile("s_waitcnt lgkmcnt(0)" ::: "memory"); __builtin_amdgcn_s_barrier(); asm volatile("" ::: "memory"); }
DI bf16x8 ld16(const void* p) { return *(const bf16x8*)p; }
DI bf16x4 pack4(f32x4 v) { bf16x4 r = {(__bf16)v[0], (__bf16)v[1], (__bf16)v[2], (__bf16)v[3]}; return r; }
DI bf16x8 cat8(bf16x4 a, bf16x4 b) { return __builtin_shufflevector(a, b, 0, 1, 2, 3, 4, 5, 6, 7); }
DI bf16x4 tr_read(const char* p) { return __builtin_amdgcn_ds_read_tr16_b64_v4bf16((LDS_AS bf16x4*)p); }
template <int CTRL> DI float dpp_mov(float v) { return __int_as_float(__builtin_amdgcn_update_dpp(0, __float_as_int(v), CTRL, 0xf, 0xf, false)); }
DI float red16_sum(float v) {
  v += dpp_mov<0x128>(v); v += dpp_mov<0x124>(v); v += dpp_mov<0x122>(v); v += dpp_mov<0x121>(v); return v;
}
DI float xmax16(float v) { auto r = __builtin_amdgcn_permlane16_swap(__float_as_uint(v), __float_as_uint(v), false, false); return fmaxf(__uint_as_float(r[0]), __uint_as_float(r[1])); }
DI float xmax32(float v) { auto r = __builtin_amdgcn_permlane32_swap(__float_as_uint(v), __float_as_uint(v), false, false); return fmaxf(__uint_as_float(r[0]), __uint_as_float(r[1])); }
DI float xsum16(float v) { auto r = __builtin_amdgcn_permlane16_swap(__float_as_uint(v), __float_as_uint(v), false, false); return __uint_as_float(r[0]) + __uint_as_float(r[1]); }
DI float xsum32(float v) { auto r = __builtin_amdgcn_permlane32_swap(__float_as_uint(v), __float_as_uint(v), false, false); return __uint_as_float(r[0]) + __uint_as_float(r[1]); }
#define SB0() __builtin_amdgcn_sched_barrier(0)

DI void ln_row(const bf16x8 (&xin)[3], bf16x8 (&hout)[3]) {
  f32x4 v[6];
#pragma unroll
  for (int i = 0; i < 3; ++i)
#pragma unroll
    for (int j = 0; j < 4; ++j) { v[2 * i][j] = (float)xin[i][j]; v[2 * i + 1][j] = (float)xin[i][4 + j]; }
  const f32x4 s4 = ((v[0] + v[1]) + (v[2] + v[3])) + (v[4] + v[5]);
  const float mean = red16_sum((s4[0] + s4[1]) + (s4[2] + s4[3])) * (1.f / 384.f);
  f32x4 q4 = {};
#pragma unroll
  for (int k = 0; k < 6; ++k) { v[k] -= mean; q4 += v[k] * v[k]; }
  const float rstd = rsqrtf(red16_sum((q4[0] + q4[1]) + (q4[2] + q4[3])) * (1.f / 384.f) + 1e-5f);
#pragma unroll
  for (int i = 0; i < 3; ++i) hout[i] = cat8(pack4(v[2 * i] * rstd), pack4(v[2 * i + 1] * rstd));
}

__global__ __launch_bounds__(256) void k0_fold(const char* __restrict__ wqkv_t, const char* __restrict__ wo_t,
                                               const char* __restrict__ wqkv_s, const char* __restrict__ wo_s,
                                               char* __restrict__ ws) {
  __shared__ __attribute__((aligned(16))) char sm[384 * 112];
  const int b = blockIdx.x, mat = (b & 7) >> 1, ti = 4 * (b & 1) + (b >> 6), tj = (b >> 3) & 7;
  const int tid = threadIdx.x, wid = __builtin_amdgcn_readfirstlane(tid >> 6), lane = tid & 63, g = lane >> 4, r16 = lane & 15;
  const char* wqkv = mat < 2 ? wqkv_t : wqkv_s;
  const char* wo = mat < 2 ? wo_t : wo_s;
  const bool isqk = (mat & 1) == 0;
  bf16x8 a[12];
  if (wid < 3) {
    const char* ap = wqkv + ((48 * ti + 16 * wid + r16) * 1152 + (isqk ? 0 : 768) + 8 * g) * 2;
#pragma unroll
    for (int ks = 0; ks < 12; ++ks) a[ks] = ld16(ap + ks * 64);
  }
  if (isqk) {
#pragma unroll
    for (int it = 0; it < 9; ++it) {
      const int idx = tid + 256 * it, row = idx / 48, ch = idx % 48;
      *(bf16x8*)(sm + row * 768 + 16 * (ch ^ swz(row & 15))) = ld16(wqkv + ((48 * tj + row) * 1152 + 384 + ch * 8) * 2);
    }
  } else {
#pragma unroll
    for (int it = 0; it < 9; ++it) {
      const int idx = tid + 256 * it, row = idx / 6, ch = idx % 6;
      *(bf16x8*)(sm + row * 112 + ch * 16) = ld16(wo + (row * 384 + 48 * tj + ch * 8) * 2);
    }
  }
  if (wid == 3 && b < 14) {
    char* dst = ws + (b < 7 ? WS_H1 : WS_QS) + ((size_t)(b % 7) * NP + NN) * ROWB;
    const u32x4 z = {0u, 0u, 0u, 0u};
    for (int i = lane; i < 7 * 48; i += 64) *(u32x4*)(dst + i * 16) = z;
  }
  __syncthreads();
  if (wid < 3) {
    f32x4 acc[3] = {};
    if (isqk) {
#pragma unroll
      for (int ks = 0; ks < 12; ++ks)
#pragma unroll
        for (int jt = 0; jt < 3; ++jt)
          acc[jt] = MFMA16(a[ks], *(const bf16x8*)(sm + (16 * jt + r16) * 768 + 16 * ((4 * ks + g) ^ swz(r16))), acc[jt]);
    } else {
      const char* tb = sm + (8 * g + (r16 >> 2)) * 112 + (lane & 3) * 8;
#pragma unroll
      for (int ks = 0; ks < 12; ++ks)
#pragma unroll
        for (int jt = 0; jt < 3; ++jt) {
          const bf16x4 lo = tr_read(tb + (32 * ks) * 112 + jt * 32), hi = tr_read(tb + (32 * ks + 4) * 112 + jt * 32);
          acc[jt] = MFMA16(a[ks], cat8(lo, hi), acc[jt]);
        }
    }
    const float sc = isqk ? QSCALE : 1.f;
    const int IT = 3 * ti + wid, ks = IT >> 1, gp = 2 * (IT & 1) + (g >> 1);
#pragma unroll
    for (int jt = 0; jt < 3; ++jt) {
      const int JT = 3 * tj + jt;
      size_t off;
      if (mat == 3) off = WS_WVOS + (size_t)(JT * 12 + ks) * 1024;
      else off = WS_W1 + (size_t)(tj * 108 + mat * 36 + ks * 3 + jt) * 1024;
      *(bf16x4*)(ws + off + (r16 + 16 * gp) * 16 + 8 * (g & 1)) = pack4(acc[jt] * sc);
    }
  }
}

constexpr int K1_D = 24, K1_A = 12;
constexpr int MIXB = 1552;
__global__ __launch_bounds__(512, 2) void k1_temporal(const char* __restrict__ x, char* __restrict__ ws) {
  __shared__ __attribute__((aligned(16))) char sm[24576 + 32 * MIXB + 16384];
  char* const Ht = sm; char* const Mx = sm + 24576; char* const Sp = sm + 24576 + 32 * MIXB;
  const int tid = threadIdx.x, wid = __builtin_amdgcn_readfirstlane(tid >> 6), lane = tid & 63, g = lane >> 4, r16 = lane & 15;
  const int n0 = blockIdx.x * 4, n = n0 + (wid >> 1);
  const int slot = 4 * wid + g, t = slot & 7;
  const bool ok = (n < NN) && (t < NT);
  bf16x8 xr[3];
  {
    const char* xp = x + ((size_t)(ok ? t * NN + n : 0)) * ROWB + r16 * 16;
#pragma unroll
    for (int i = 0; i < 3; ++i) xr[i] = ld16(xp + i * 256);
  }
  SB0();
  const char* wp = ws + WS_W1 + (size_t)wid * 108 * 1024 + lane * 16;
  const int rot = (blockIdx.x >> 3) % 12, rot2 = ((blockIdx.x >> 3) / 12 + blockIdx.x) % 3;
  int kso[12], c3o[3];
#pragma unroll
  for (int j = 0; j < 12; ++j) kso[j] = (j + rot) % 12;
#pragma unroll
  for (int j = 0; j < 3; ++j) c3o[j] = (j + rot2) % 3;
#define WOFF(s) ((((s) / 36) * 36 + kso[((s) % 36) / 3] * 3 + c3o[(s) % 3]) * 1024)
  bf16x8 w[K1_D];
#pragma unroll
  for (int s = 0; s < 3; ++s) w[s] = ld16(wp + WOFF(s));
  SB0();
  {
    if (!ok) { xr[0] = bf16x8{}; xr[1] = bf16x8{}; xr[2] = bf16x8{}; }
    bf16x8 h[3]; ln_row(xr, h);
#pragma unroll
    for (int i = 0; i < 3; ++i) *(bf16x8*)(Ht + slot * ROWB + 16 * ((r16 + 16 * i) ^ swz(slot & 15))) = h[i];
    asm volatile("" : "+v"(xr[0]), "+v"(xr[1]), "+v"(xr[2]));
  }
  SB0();
#pragma unroll
  for (int s = 3; s < K1_A; ++s) w[s] = ld16(wp + WOFF(s));
  lds_barrier();
  f32x4 acc[3][2];
#define HFRAG(u, jk) (*(const bf16x8*)(Ht + (16 * (u) + r16) * ROWB + 16 * ((4 * kso[jk] + g) ^ swz(r16))))
#define K1_GEMM(MAT, TR, HOOK) do {                                                        \
    bf16x8 hb[2][2];                                                                        \
    hb[0][0] = HFRAG(0, 0); hb[0][1] = HFRAG(1, 0);                                         \
    _Pragma("unroll") for (int c = 0; c < 3; ++c) { acc[c][0] = f32x4{}; acc[c][1] = f32x4{}; } \
    _Pragma("unroll") for (int jk = 0; jk < 12; ++jk) {                                     \
      if (jk < 11) { hb[(jk + 1) & 1][0] = HFRAG(0, jk + 1); hb[(jk + 1) & 1][1] = HFRAG(1, jk + 1); } \
      HOOK;                                                                                 \
      _Pragma("unroll") for (int c = 0; c < 3; ++c) {                                       \
        const int s = (MAT) * 36 + jk * 3 + c;                                              \
        const bf16x8 wf = w[s % K1_D];                                                      \
        if ((MAT) < 2) w[(s + K1_A) % K1_D] = ld16(wp + WOFF(s + K1_A));                    \
        else if (s + K1_D < 108) w[s % K1_D] = ld16(wp + WOFF(s + K1_D));                   \
        if (TR) { acc[c][0] = MFMA16(wf, hb[jk & 1][0], acc[c][0]); acc[c][1] = MFMA16(wf, hb[jk & 1][1], acc[c][1]); } \
        else    { acc[c][0] = MFMA16(hb[jk & 1][0], wf, acc[c][0]); acc[c][1] = MFMA16(hb[jk & 1][1], wf, acc[c][1]); } \
        SB0();                                                                              \
      }                                                                                     \
    }                                                                                       \
  } while (0)

  K1_GEMM(0, true, );
#pragma unroll
  for (int u = 0; u < 2; ++u) {
    bf16x4 hs[3];
#pragma unroll
    for (int c = 0; c < 3; ++c)
      hs[c] = *(const bf16x4*)(Ht + (16 * u + r16) * ROWB + 8 * (g & 1) + 16 * ((6 * wid + 2 * c3o[c] + (g >> 1)) ^ swz(r16)));
    const bf16x4 z = {};
    f32x4 sacc = MFMA16(cat8(hs[0], hs[1]), cat8(pack4(acc[0][u]), pack4(acc[1][u])), f32x4{});
    sacc = MFMA16(cat8(hs[2], z), cat8(pack4(acc[2][u]), z), sacc);
    *(f32x4*)(Sp + (wid * 2 + u) * 1024 + lane * 16) = sacc;
  }
  SB0();
  bf16x8 pf[2];
  auto tsoftmax = [&](int u) {
    const bool blk = (g >> 1) == (r16 >> 3);
    f32x4 sp[8];
#pragma unroll
    for (int ww = 0; ww < 8; ++ww) sp[ww] = *(const f32x4*)(Sp + (ww * 2 + u) * 1024 + lane * 16);
    f32x4 s = ((sp[0] + sp[1]) + (sp[2] + sp[3])) + ((sp[4] + sp[5]) + (sp[6] + sp[7]));
    if (g & 1) s[3] = NEG;
    const float mx = xmax16(fmaxf(fmaxf(s[0], s[1]), fmaxf(s[2], s[3])));
    f32x4 p;
#pragma unroll
    for (int r = 0; r < 4; ++r) p[r] = __builtin_amdgcn_exp2f(s[r] - mx);
    const float sum = xsum16((p[0] + p[1]) + (p[2] + p[3]));
    const float inv = blk ? __builtin_amdgcn_rcpf(sum) : 0.f;
    const bf16x4 pb = pack4(p * inv), z = {};
    pf[u] = u == 0 ? cat8(pb, z) : cat8(z, pb);
  };
  K1_GEMM(1, false, { if (jk == 5) lds_barrier(); if (jk == 6) tsoftmax(0); if (jk == 8) tsoftmax(1); });
  {
#pragma unroll
    for (int c = 0; c < 3; ++c) {
      const bf16x8 vf = cat8(pack4(acc[c][0]), pack4(acc[c][1]));
#pragma unroll
      for (int u = 0; u < 2; ++u)
        *(f32x4*)(Mx + (16 * u + r16) * MIXB + (48 * wid + 16 * c3o[c] + 4 * g) * 4) = MFMA16(vf, pf[u], f32x4{});
    }
  }
  SB0();
#pragma unroll
  for (int s = 84; s < 90; ++s) w[s % K1_D] = ld16(wp + WOFF(s));
  lds_barrier();
  bf16x8 x1[3], h1[3];
  {
#pragma unroll
    for (int i = 0; i < 3; ++i) {
      f32x4 m0 = *(const f32x4*)(Mx + slot * MIXB + (r16 + 16 * i) * 32), m1 = *(const f32x4*)(Mx + slot * MIXB + (r16 + 16 * i) * 32 + 16);
#pragma unroll
      for (int j = 0; j < 4; ++j) { m0[j] += (float)xr[i][j]; m1[j] += (float)xr[i][4 + j]; }
      x1[i] = cat8(pack4(m0), pack4(m1));
    }
    ln_row(x1, h1);
#pragma unroll
    for (int i = 0; i < 3; ++i) *(bf16x8*)(Ht + slot * ROWB + 16 * ((r16 + 16 * i) ^ swz(slot & 15))) = h1[i];
  }
  SB0();
#pragma unroll
  for (int s = 90; s < 96; ++s) w[s % K1_D] = ld16(wp + WOFF(s));
  auto put_rows = [&]() {
    if (ok) {
      char* xo = ws + WS_X1 + (size_t)(t * NN + n) * ROWB + r16 * 16;
      char* ho = ws + WS_H1 + (size_t)(t * NP + n) * ROWB;
#pragma unroll
      for (int i = 0; i < 3; ++i) {
        __builtin_nontemporal_store(x1[i], (bf16x8*)(xo + i * 256));
        __builtin_nontemporal_store(h1[i], (bf16x8*)(ho + 16 * ((r16 + 16 * i) ^ swz(n & 15))));
      }
    }
  };
  lds_barrier();
  K1_GEMM(2, true, { if (jk == 5) put_rows(); });
#pragma unroll
  for (int u = 0; u < 2; ++u)
#pragma unroll
    for (int c = 0; c < 3; ++c) *(bf16x4*)(Mx + (16 * u + r16) * 800 + (48 * wid + 16 * c3o[c] + 4 * g) * 2) = pack4(acc[c][u]);
  lds_barrier();
  if (ok) {
    char* qo = ws + WS_QS + (size_t)(t * NP + n) * ROWB;
#pragma unroll
    for (int i = 0; i < 3; ++i)
      __builtin_nontemporal_store(*(const bf16x8*)(Mx + slot * 800 + (r16 + 16 * i) * 16), (bf16x8*)(qo + 16 * ((r16 + 16 * i) ^ swz(n & 15))));
  }
}

constexpr int SLOT = 24576;
constexpr int K2_P = 6 * SLOT;
constexpr int K2_MX = K2_P + 8192;
constexpr int K2_L = K2_MX + 512;
constexpr int K2_AL = K2_L + 512;
constexpr int K2_FL = K2_AL + 128;
constexpr int K2_LDS = K2_FL + 64;
constexpr float T_BIG = 1.8446744e19f;

DI void dma6(const char* src, int dst, int vo) {
  asm volatile("s_mov_b32 m0, %3\n\ts_nop 0\n\t"
               "global_load_lds_dwordx4 %0, %2\n\tglobal_load_lds_dwordx4 %0, %2 offset:1024\n\t"
               "global_load_lds_dwordx4 %0, %2 offset:2048\n\tglobal_load_lds_dwordx4 %0, %2 offset:3072\n\t"
               "s_mov_b32 m0, %4\n\ts_nop 0\n\t"
               "global_load_lds_dwordx4 %1, %2\n\tglobal_load_lds_dwordx4 %1, %2 offset:1024"
               :: "v"(vo), "v"(vo + 4096), "s"(src), "s"(dst), "s"(dst + 4096) : "memory");
}
#define VMWAIT(n) asm volatile("s_waitcnt vmcnt(" #n ")" ::: "memory")
DI float vmax(float a, float b) { float r; asm("v_max_f32 %0, %1, %2" : "=v"(r) : "v"(a), "v"(b)); return r; }
DI float vmax3(float a, float b, float c) { float r; asm("v_max3_f32 %0, %1, %2, %3" : "=v"(r) : "v"(a), "v"(b), "v"(c)); return r; }
DI float xm16(float v) { auto r = __builtin_amdgcn_permlane16_swap(__float_as_uint(v), __float_as_uint(v), false, false); return vmax(__uint_as_float(r[0]), __uint_as_float(r[1])); }
DI float xm32(float v) { auto r = __builtin_amdgcn_permlane32_swap(__float_as_uint(v), __float_as_uint(v), false, false); return vmax(__uint_as_float(r[0]), __uint_as_float(r[1])); }
#define LDSU(off) (*(volatile LDS_AS unsigned*)(sm + (off)))

__global__ __launch_bounds__(768, 3) void k2_spatial(const char* __restrict__ ws, char* __restrict__ out) {
  extern __shared__ __attribute__((aligned(16))) char sm[];
  const int f = blockIdx.x & 7, qt = blockIdx.x >> 3;
  if (f >= NT) return;
  const int tid = threadIdx.x, wid = __builtin_amdgcn_readfirstlane(tid >> 6), lane = tid & 63, g = lane >> 4, r16 = lane & 15;
  const int w4 = wid & 3;
  const char* wv = ws + WS_WVOS;
  bf16x8 wreg[24];
  const int rotw = qt % 12;
#define WFR(c, j) ld16(wv + wid * SLOT + ((c) * 12 + ((j) + rotw) % 12) * 1024 + lane * 16)
  const int dvo = lane * 16 + w4 * 6144, dq = w4 * 6144;

  if (wid >= 8) {
    const char* kv = ws + WS_H1 + (size_t)f * NP * ROWB;
#define KVH(k) (kv + ((qt + (k)) % 23) * SLOT)
#define BAR() do { __builtin_amdgcn_s_barrier(); asm volatile("" ::: "memory"); } while (0)
    dma6(ws + WS_QS + (size_t)(f * NP + 32 * qt) * ROWB, 4 * SLOT + dq, dvo);
    dma6(KVH(0), dq, dvo); dma6(KVH(1), SLOT + dq, dvo);
    dma6(KVH(2), 2 * SLOT + dq, dvo); dma6(KVH(3), 3 * SLOT + dq, dvo);
    if (w4 == 0 && lane < 16) LDSU(K2_FL + 4 * lane) = 0u;
    VMWAIT(12); lds_barrier();
    BAR();
    dma6(KVH(4), 4 * SLOT + dq, dvo);
    VMWAIT(6);
#pragma unroll
    for (int i = 1; i <= 12; ++i) {
      BAR();
      const int fb = ((i + 2) % 3) * 2 * SLOT;
      const unsigned fl = LDSU(K2_FL + 4 * (i - 1));
      if (i == 1) { dma6(KVH(5), 5 * SLOT + dq, dvo); dma6(KVH(6), dq, dvo); dma6(KVH(7), SLOT + dq, dvo); }
      if (i >= 2 && i <= 11) {
        dma6(i <= 9 ? KVH(2 * i + 4) : wv + (2 * i - 16) * SLOT, fb + dq, dvo);
        if (i != 9) dma6(i <= 8 ? KVH(2 * i + 5) : wv + (2 * i - 15) * SLOT, fb + SLOT + dq, dvo);
      }
      if (i >= 2 && i <= 9) {
#pragma unroll
        for (int j = 3 * (i - 2); j < 3 * (i - 1); ++j) wreg[j] = WFR(j / 12, j % 12);
      }
      if (i >= 2) { if (__builtin_amdgcn_readfirstlane(fl)) { BAR(); BAR(); } }
      if (i == 1 || i == 10) VMWAIT(12); else if (i == 9) VMWAIT(9); else if (i < 9) VMWAIT(15);
    }
    VMWAIT(0);
  } else
  if (wid < 4) {
    __builtin_amdgcn_s_setprio(2);
    const int fsw = swz(r16);
    int roff[2][4];
#pragma unroll
    for (int b = 0; b < 4; ++b) { roff[0][b] = (16 * w4 + r16) * ROWB + 16 * ((4 * b + g) ^ fsw); roff[1][b] = roff[0][b] + 4 * SLOT; }
#define KFRAG(tbuf, ks) (*(const bf16x8*)(sm + roff[(tbuf) >> 1][(ks) & 3] + ((tbuf) & 1) * 2 * SLOT + ((ks) >> 2) * 256))
    const int pw_off = K2_P + r16 * 128 + 16 * ((2 * w4 + (g >> 1)) ^ (r16 & 7)) + 8 * (g & 1);
    const int kpad = (w4 & 1) ? 22 - qt : -1;
    bf16x8 qf[2][12];
    float R[2], l[2] = {0.f, 0.f}, ls[2];
    f32x4 s[2];
    auto mask_u = [&](int i, int u) {
      const int kidx = 2 * i + (w4 >> 1);
      if (kidx >= 23 || kidx == kpad) {
#pragma unroll
        for (int r = 0; r < 4; ++r) s[u][r] = (kidx >= 23 || 4 * g + r >= 9) ? NEG : s[u][r];
      }
    };
    auto put_p = [&](int i) {
      f32x4 p[2];
#pragma unroll
      for (int r = 0; r < 4; ++r) { p[0][r] = __builtin_amdgcn_exp2f(s[0][r]); p[1][r] = __builtin_amdgcn_exp2f(s[1][r]); }
      *(bf16x4*)(sm + (i & 1) * 4096 + pw_off) = pack4(p[0]); *(bf16x4*)(sm + (i & 1) * 4096 + 2048 + pw_off) = pack4(p[1]);
      ls[0] = (p[0][0] + p[0][1]) + (p[0][2] + p[0][3]); ls[1] = (p[1][0] + p[1][1]) + (p[1][2] + p[1][3]);
    };
    auto put_max = [&]() {
      const float m0 = xm32(xm16(vmax(vmax3(s[0][0], s[0][1], s[0][2]), s[0][3]))), m1 = xm32(xm16(vmax(vmax3(s[1][0], s[1][1], s[1][2]), s[1][3])));
      if (g == 0) { *(float*)(sm + K2_MX + r16 * 16 + w4 * 4) = m0; *(float*)(sm + K2_MX + (16 + r16) * 16 + w4 * 4) = m1; }
    };
    auto get_max = [&](float (&M)[2]) {
#pragma unroll
      for (int u = 0; u < 2; ++u) {
        const f32x4 mm = *(const f32x4*)(sm + K2_MX + (16 * u + r16) * 16);
        M[u] = vmax(vmax3(mm[0], mm[1], mm[2]), mm[3]);
      }
    };
    auto redo = [&](int i) {
      put_max();
      lds_barrier();
      float M[2], a[2]; get_max(M);
#pragma unroll
      for (int u = 0; u < 2; ++u) { const float d = vmax(M[u], 0.f); a[u] = __builtin_amdgcn_exp2f(-d); R[u] += d; l[u] *= a[u]; s[u] -= d; }
      put_p(i - 1);
      if (wid == 0 && g == 0) { *(float*)(sm + K2_AL + r16 * 4) = a[0]; *(float*)(sm + K2_AL + 64 + r16 * 4) = a[1]; }
      lds_barrier();
    };

    lds_barrier();
    {
      bf16x8 kf[12];
#pragma unroll
      for (int ks = 0; ks < 12; ++ks) {
        kf[ks] = *(const bf16x8*)(sm + roff[0][ks & 3] + (ks >> 2) * 256);
        qf[0][ks] = *(const bf16x8*)(sm + roff[1][ks & 3] - 16 * w4 * ROWB + (ks >> 2) * 256);
        qf[1][ks] = *(const bf16x8*)(sm + roff[1][ks & 3] - 16 * w4 * ROWB + 16 * ROWB + (ks >> 2) * 256);
      }
      s[0] = f32x4{}; s[1] = f32x4{};
#pragma unroll
      for (int ks = 0; ks < 12; ++ks) { s[0] = MFMA16(kf[ks], qf[0][ks], s[0]); s[1] = MFMA16(kf[ks], qf[1][ks], s[1]); }
      mask_u(0, 0); mask_u(0, 1);
      put_max();
    }
    lds_barrier();
    get_max(R);
    s[0] -= R[0]; s[1] -= R[1];
    put_p(0);
#pragma unroll
    for (int i = 1; i <= 11; ++i) {
      lds_barrier();
      bf16x8 kf[6];
      const unsigned fl = LDSU(K2_FL + 4 * (i - 1));
#pragma unroll
      for (int ks = 0; ks < 6; ++ks) kf[ks] = KFRAG(i % 3, ks);
      if (i > 1) { if (__builtin_amdgcn_readfirstlane(fl)) redo(i); }
      l[0] += ls[0]; l[1] += ls[1];
      SB0();
      s[0] = f32x4{-R[0], -R[0], -R[0], -R[0]}; s[1] = f32x4{-R[1], -R[1], -R[1], -R[1]};
#pragma unroll
      for (int ks = 0; ks < 12; ++ks) {
        s[0] = MFMA16(kf[ks % 6], qf[0][ks], s[0]);
        s[1] = MFMA16(kf[ks % 6], qf[1][ks], s[1]);
        if (ks < 6) kf[ks] = KFRAG(i % 3, 6 + ks);
        SB0();
      }
      mask_u(i, 0); mask_u(i, 1);
      put_p(i);
      if (__any(!(ls[0] + ls[1] < T_BIG))) { if (lane == 0) LDSU(K2_FL + 4 * i) = 1u; }
    }
    lds_barrier();
    if (__builtin_amdgcn_readfirstlane(LDSU(K2_FL + 4 * 11))) redo(12);
#pragma unroll
    for (int u = 0; u < 2; ++u) {
      const float t = xsum32(xsum16(l[u] + ls[u]));
      if (g == 0) *(float*)(sm + K2_L + (16 * u + r16) * 16 + w4 * 4) = t;
    }
    __builtin_amdgcn_s_setprio(0);
#pragma unroll
    for (int j = 0; j < 12; ++j) { wreg[j] = WFR(0, j); wreg[12 + j] = WFR(1, j); }
  } else {
    const int vsw = (8 * (g & 1)) | ((r16 >> 2) << 1);
    int voff[2][6];
#pragma unroll
    for (int i = 0; i < 6; ++i) {
      voff[0][i] = (8 * g + (r16 >> 2)) * ROWB + 16 * ((12 * w4 + 2 * i + ((lane & 3) >> 1)) ^ vsw) + 8 * (lane & 1);
      voff[1][i] = voff[0][i] + 3 * SLOT;
    }
    int pr_off[2];
#pragma unroll
    for (int hh = 0; hh < 2; ++hh) pr_off[hh] = K2_P + r16 * 128 + 16 * ((4 * hh + g) ^ (r16 & 7));
    f32x4 o[6][2];
#pragma unroll
    for (int i = 0; i < 6; ++i) { o[i][0] = f32x4{}; o[i][1] = f32x4{}; }
    bf16x8 vf[2][6];
    auto ld_v = [&](int tbuf, int hh, int c) {
      const int sl = 2 * tbuf + hh;
      return cat8(tr_read(sm + voff[sl / 3][c] + (sl % 3) * SLOT), tr_read(sm + voff[sl / 3][c] + (sl % 3) * SLOT + 4 * ROWB));
    };
    auto redo_wait = [&]() {
      lds_barrier(); lds_barrier();
      const float a0 = *(const float*)(sm + K2_AL + r16 * 4), a1 = *(const float*)(sm + K2_AL + 64 + r16 * 4);
#pragma unroll
      for (int c = 0; c < 6; ++c) { o[c][0] *= a0; o[c][1] *= a1; }
    };
    lds_barrier();
#pragma unroll
    for (int c = 0; c < 6; ++c) { vf[0][c] = ld_v(0, 0, c); vf[1][c] = ld_v(0, 1, c); }
    lds_barrier();
#pragma unroll
    for (int i = 1; i <= 11; ++i) {
      lds_barrier();
      const int sp = ((i - 1) & 1) * 4096;
      const unsigned fl = LDSU(K2_FL + 4 * (i - 1));
      bf16x8 pf[2][2];
#pragma unroll
      for (int hh = 0; hh < 2; ++hh) { pf[hh][0] = *(const bf16x8*)(sm + sp + pr_off[hh]); pf[hh][1] = *(const bf16x8*)(sm + sp + pr_off[hh] + 2048); }
      if (i > 1) {
        if (__builtin_amdgcn_readfirstlane(fl)) {
          redo_wait();
#pragma unroll
          for (int hh = 0; hh < 2; ++hh) { pf[hh][0] = *(const bf16x8*)(sm + sp + pr_off[hh]); pf[hh][1] = *(const bf16x8*)(sm + sp + pr_off[hh] + 2048); }
        }
      }
      SB0();
#pragma unroll
      for (int hh = 0; hh < 2; ++hh)
#pragma unroll
        for (int c = 0; c < 6; ++c) {
          o[c][0] = MFMA16(vf[hh][c], pf[hh][0], o[c][0]);
          o[c][1] = MFMA16(vf[hh][c], pf[hh][1], o[c][1]);
          if (i < 11 || hh == 0) vf[hh][c] = ld_v(i % 3, hh, c);
          SB0();
        }
    }
    lds_barrier();
    if (__builtin_amdgcn_readfirstlane(LDSU(K2_FL + 4 * 11))) redo_wait();
    {
      const bf16x8 p0 = *(const bf16x8*)(sm + 4096 + pr_off[0]), p1 = *(const bf16x8*)(sm + 4096 + pr_off[0] + 2048);
#pragma unroll
      for (int c = 0; c < 6; ++c) { o[c][0] = MFMA16(vf[0][c], p0, o[c][0]); o[c][1] = MFMA16(vf[0][c], p1, o[c][1]); }
    }
    int lanee = __builtin_amdgcn_mbcnt_hi(-1, __builtin_amdgcn_mbcnt_lo(-1, 0));
    asm volatile("" : "+v"(lanee));
    const int ge = lanee >> 4, r16e = lanee & 15;
#pragma unroll
    for (int c = 0; c < 6; ++c) {
      char* op = sm + 5 * SLOT + r16e * ROWB + 16 * ((12 * w4 + 2 * c + (ge >> 1)) ^ swz(r16e)) + 8 * (ge & 1);
      *(bf16x4*)op = pack4(o[c][0]); *(bf16x4*)(op + 16 * ROWB) = pack4(o[c][1]);
    }
  }

  int lanet = __builtin_amdgcn_mbcnt_hi(-1, __builtin_amdgcn_mbcnt_lo(-1, 0));
  asm volatile("" : "+v"(lanet));
  const int gt = lanet >> 4, r16t = lanet & 15;
  bf16x4 xres[2][2];
#pragma unroll
  for (int u = 0; u < 2; ++u) {
    const int q = 32 * qt + 16 * u + r16t;
    const char* xp = ws + WS_X1 + (size_t)(f * NN + (q < NN ? q : 0)) * ROWB + (32 * wid + 4 * gt) * 2;
    xres[0][u] = *(const bf16x4*)xp; xres[1][u] = *(const bf16x4*)(xp + 32);
  }
  int ooff[4];
#pragma unroll
  for (int b = 0; b < 4; ++b) ooff[b] = 5 * SLOT + r16t * ROWB + 16 * ((4 * b + gt) ^ swz(r16t));
  lds_barrier();
  f32x4 acc[2][2] = {};
  float inv[2];
#pragma unroll
  for (int u = 0; u < 2; ++u) {
    const f32x4 ll = *(const f32x4*)(sm + K2_L + (16 * u + r16t) * 16);
    inv[u] = __builtin_amdgcn_rcpf((ll[0] + ll[1]) + (ll[2] + ll[3]));
  }
#define OFRAG(u, ks) (*(const bf16x8*)(sm + ooff[(ks) & 3] + (u) * 16 * ROWB + ((ks) >> 2) * 256))
  if (wid >= 4 && wid < 8) {
    const char* wl = sm + w4 * SLOT + lanet * 16;
#pragma unroll
    for (int ks = 0; ks < 12; ++ks) {
      const bf16x8 o0 = OFRAG(0, ks), o1 = OFRAG(1, ks), w0 = *(const bf16x8*)(wl + ks * 1024), w1 = *(const bf16x8*)(wl + (12 + ks) * 1024);
      acc[0][0] = MFMA16(w0, o0, acc[0][0]); acc[0][1] = MFMA16(w0, o1, acc[0][1]);
      acc[1][0] = MFMA16(w1, o0, acc[1][0]); acc[1][1] = MFMA16(w1, o1, acc[1][1]);
    }
  } else {
#pragma unroll
    for (int ks = 0; ks < 12; ++ks) {
      const char* ob = sm + 5 * SLOT + r16t * ROWB + 16 * ((4 * ((ks + rotw) % 12) + gt) ^ swz(r16t));
      const bf16x8 o0 = *(const bf16x8*)ob, o1 = *(const bf16x8*)(ob + 16 * ROWB);
      acc[0][0] = MFMA16(wreg[ks], o0, acc[0][0]); acc[0][1] = MFMA16(wreg[ks], o1, acc[0][1]);
      acc[1][0] = MFMA16(wreg[12 + ks], o0, acc[1][0]); acc[1][1] = MFMA16(wreg[12 + ks], o1, acc[1][1]);
    }
  }
#pragma unroll
  for (int u = 0; u < 2; ++u) {
    const int q = 32 * qt + 16 * u + r16t;
    if (q < NN) {
      char* op = out + (size_t)(f * NN + q) * ROWB + (32 * wid + 4 * gt) * 2;
#pragma unroll
      for (int c = 0; c < 2; ++c) {
        f32x4 v = acc[c][u];
#pragma unroll
        for (int r = 0; r < 4; ++r) v[r] = v[r] * inv[u] + (float)xres[c][u][r];
        *(bf16x4*)(op + c * 32) = pack4(v);
      }
    }
  }
}

}

extern "C" void kernel_launch(void* const* d_in, const int* in_sizes, int n_in,
                              void* d_out, int out_size, void* d_ws, size_t ws_size,
                              hipStream_t stream) {
  (void)in_sizes; (void)n_in; (void)out_size; (void)ws_size;
  const char* x = (const char*)d_in[0];
  char* ws = (char*)d_ws;
  hipFuncSetAttribute((const void*)k2_spatial, hipFuncAttributeMaxDynamicSharedMemorySize, K2_LDS);
  k0_fold<<<256, 256, 0, stream>>>((const char*)d_in[1], (const char*)d_in[2], (const char*)d_in[3], (const char*)d_in[4], ws);
  k1_temporal<<<183, 512, 0, stream>>>(x, ws);
  k2_spatial<<<184, 768, K2_LDS, stream>>>(ws, (char*)d_out);
}
```

```cpp
#include <hip/hip_runtime.h>
#include <hip/hip_bf16.h>
#include <stdint.h>

typedef __attribute__((ext_vector_type(8))) __bf16 bf16x8;
typedef __attribute__((ext_vector_type(4))) __bf16 bf16x4;
typedef __attribute__((ext_vector_type(4))) float f32x4;
typedef __attribute__((ext_vector_type(4))) unsigned u32x4;
#define LDS_AS __attribute__((address_space(3)))
#define DI __device__ __forceinline__
#define MFMA16(a, b, c) __builtin_amdgcn_mfma_f32_16x16x32_bf16((a), (b), (c), 0, 0, 0)

namespace {

constexpr int NT = 7, NN = 729, NP = 736;
constexpr int ROWB = 768;
constexpr size_t WS_W1   = 0;
constexpr size_t WS_WVOS = 884736;
constexpr size_t WS_ST   = WS_WVOS + 294912;
constexpr size_t WS_H1   = WS_ST + 45056;
constexpr float QSCALE = 0.05103103630798288f * 1.4426950408889634f;
constexpr float NEG = -1e30f;

DI int swz(int k) { return (k & 8) | ((k & 3) << 1); }
DI void lds_barrier() { asm volatile("s_waitcnt lgkmcnt(0)" ::: "memory"); __builtin_amdgcn_s_barrier(); asm volatile("" ::: "memory"); }
DI bf16x8 ld16(const void* p) { return *(const bf16x8*)p; }
DI bf16x4 pack4(f32x4 v) { bf16x4 r = {(__bf16)v[0], (__bf16)v[1], (__bf16)v[2], (__bf16)v[3]}; return r; }
DI bf16x8 cat8(bf16x4 a, bf16x4 b) { return __builtin_shufflevector(a, b, 0, 1, 2, 3, 4, 5, 6, 7); }
DI bf16x4 tr_read(const char* p) { return __builtin_amdgcn_ds_read_tr16_b64_v4bf16((LDS_AS bf16x4*)p); }
template <int CTRL> DI float dpp_mov(float v) { return __int_as_float(__builtin_amdgcn_update_dpp(0, __float_as_int(v), CTRL, 0xf, 0xf, false)); }
DI float red16_sum(float v) {
  v += dpp_mov<0x128>(v); v += dpp_mov<0x124>(v); v += dpp_mov<0x122>(v); v += dpp_mov<0x121>(v); return v;
}
DI float xmax16(float v) { auto r = __builtin_amdgcn_permlane16_swap(__float_as_uint(v), __float_as_uint(v), false, false); return fmaxf(__uint_as_float(r[0]), __uint_as_float(r[1])); }
DI float xmax32(float v) { auto r = __builtin_amdgcn_permlane32_swap(__float_as_uint(v), __float_as_uint(v), false, false); return fmaxf(__uint_as_float(r[0]), __uint_as_float(r[1])); }
DI float xsum16(float v) { auto r = __builtin_amdgcn_permlane16_swap(__float_as_uint(v), __float_as_uint(v), false, false); return __uint_as_float(r[0]) + __uint_as_float(r[1]); }
DI float xsum32(float v) { auto r = __builtin_amdgcn_permlane32_swap(__float_as_uint(v), __float_as_uint(v), false, false); return __uint_as_float(r[0]) + __uint_as_float(r[1]); }
#define SB0() __builtin_amdgcn_sched_barrier(0)

DI void ln_row(const bf16x8 (&xin)[3], bf16x8 (&hout)[3], float& mean_o, float& std_o) {
  f32x4 v[6];
#pragma unroll
  for (int i = 0; i < 3; ++i)
#pragma unroll
    for (int j = 0; j < 4; ++j) { v[2 * i][j] = (float)xin[i][j]; v[2 * i + 1][j] = (float)xin[i][4 + j]; }
  const f32x4 s4 = ((v[0] + v[1]) + (v[2] + v[3])) + (v[4] + v[5]);
  const float mean = red16_sum((s4[0] + s4[1]) + (s4[2] + s4[3])) * (1.f / 384.f);
  f32x4 q4 = {};
#pragma unroll
  for (int k = 0; k < 6; ++k) { v[k] -= mean; q4 += v[k] * v[k]; }
  const float ve = red16_sum((q4[0] + q4[1]) + (q4[2] + q4[3])) * (1.f / 384.f) + 1e-5f, rstd = rsqrtf(ve);
  mean_o = mean; std_o = ve * rstd;
#pragma unroll
  for (int i = 0; i < 3; ++i) hout[i] = cat8(pack4(v[2 * i] * rstd), pack4(v[2 * i + 1] * rstd));
}

__global__ __launch_bounds__(256) void k0_fold(const char* __restrict__ wqkv_t, const char* __restrict__ wo_t,
                                               const char* __restrict__ wqkv_s, const char* __restrict__ wo_s,
                                               char* __restrict__ ws, const char* __restrict__ x, const char* __restrict__ out) {
  __shared__ __attribute__((aligned(16))) char sm[384 * 112];
  const int b = blockIdx.x, mat = (b & 7) >> 1, ti = 4 * (b & 1) + (b >> 6), tj = (b >> 3) & 7;
  const int tid = threadIdx.x, wid = __builtin_amdgcn_readfirstlane(tid >> 6), lane = tid & 63, g = lane >> 4, r16 = lane & 15;
  const char* wqkv = mat < 2 ? wqkv_t : wqkv_s;
  const char* wo = mat < 2 ? wo_t : wo_s;
  const bool isqk = (mat & 1) == 0;
  bf16x8 a[12];
  if (wid < 3) {
    const char* ap = wqkv + ((48 * ti + 16 * wid + r16) * 1152 + (isqk ? 0 : 768) + 8 * g) * 2;
#pragma unroll
    for (int ks = 0; ks < 12; ++ks) a[ks] = ld16(ap + ks * 64);
  }
  if (isqk) {
#pragma unroll
    for (int it = 0; it < 9; ++it) {
      const int idx = tid + 256 * it, row = idx / 48, ch = idx % 48;
      *(bf16x8*)(sm + row * 768 + 16 * (ch ^ swz(row & 15))) = ld16(wqkv + ((48 * tj + row) * 1152 + 384 + ch * 8) * 2);
    }
  } else {
#pragma unroll
    for (int it = 0; it < 9; ++it) {
      const int idx = tid + 256 * it, row = idx / 6, ch = idx % 6;
      *(bf16x8*)(sm + row * 112 + ch * 16) = ld16(wo + (row * 384 + 48 * tj + ch * 8) * 2);
    }
  }
  if (wid == 3 && b < 7) {
    char* dst = ws + WS_H1 + ((size_t)b * NP + NN) * ROWB;
    const u32x4 z = {0u, 0u, 0u, 0u};
    for (int i = lane; i < 7 * 48; i += 64) *(u32x4*)(dst + i * 16) = z;
  }
  if (wid == 3 && b >= 8 && b < 72) {
    const int k = ((b >> 3) - 1) * 64 + lane;
    const unsigned v0 = *(const unsigned*)(x + (size_t)(k < 478 ? k : 478) * 8192), v1 = *(const unsigned*)(out + (size_t)(k < 478 ? k : 478) * 8192);
    const unsigned v2 = *(const unsigned*)(ws + WS_H1 + (size_t)(k < 482 ? k : 482) * 8192);
    asm volatile("" :: "v"(v0), "v"(v1), "v"(v2));
  }
  __syncthreads();
  if (wid < 3) {
    f32x4 acc[3] = {};
    if (isqk) {
#pragma unroll
      for (int ks = 0; ks < 12; ++ks)
#pragma unroll
        for (int jt = 0; jt < 3; ++jt)
          acc[jt] = MFMA16(a[ks], *(const bf16x8*)(sm + (16 * jt + r16) * 768 + 16 * ((4 * ks + g) ^ swz(r16))), acc[jt]);
    } else {
      const char* tb = sm + (8 * g + (r16 >> 2)) * 112 + (lane & 3) * 8;
#pragma unroll
      for (int ks = 0; ks < 12; ++ks)
#pragma unroll
        for (int jt = 0; jt < 3; ++jt) {
          const bf16x4 lo = tr_read(tb + (32 * ks) * 112 + jt * 32), hi = tr_read(tb + (32 * ks + 4) * 112 + jt * 32);
          acc[jt] = MFMA16(a[ks], cat8(lo, hi), acc[jt]);
        }
    }
    const float sc = isqk ? QSCALE : 1.f;
    const int IT = 3 * ti + wid, ks = IT >> 1, gp = 2 * (IT & 1) + (g >> 1);
#pragma unroll
    for (int jt = 0; jt < 3; ++jt) {
      const int JT = 3 * tj + jt;
      size_t off;
      if (mat == 3) off = WS_WVOS + (size_t)(JT * 12 + ks) * 1024;
      else off = WS_W1 + (size_t)(tj * 108 + mat * 36 + ks * 3 + jt) * 1024;
      *(bf16x4*)(ws + off + (r16 + 16 * gp) * 16 + 8 * (g & 1)) = pack4(acc[jt] * sc);
    }
  }
}

constexpr int K1_D = 12;
constexpr int MIXB = 1552;
__global__ __launch_bounds__(512, 2) void k1_temporal(const char* __restrict__ x, char* __restrict__ ws) {
  __shared__ __attribute__((aligned(16))) char sm[24576 + 32 * MIXB + 16384];
  char* const Ht = sm; char* const Mx = sm + 24576; char* const Sp = sm + 24576 + 32 * MIXB;
  const int tid = threadIdx.x, wid = __builtin_amdgcn_readfirstlane(tid >> 6), lane = tid & 63, g = lane >> 4, r16 = lane & 15;
  const int n0 = blockIdx.x * 4, n = n0 + (wid >> 1);
  const int slot = 4 * wid + g, t = slot & 7;
  const bool ok = (n < NN) && (t < NT);
  bf16x8 xr[3];
  {
    const char* xp = x + ((size_t)(ok ? t * NN + n : 0)) * ROWB + r16 * 16;
#pragma unroll
    for (int i = 0; i < 3; ++i) xr[i] = ld16(xp + i * 256);
  }
  SB0();
  const char* wp = ws + WS_W1 + (size_t)wid * 108 * 1024 + lane * 16;
  const int rot = (blockIdx.x >> 3) % 12, rot2 = ((blockIdx.x >> 3) / 12 + blockIdx.x) % 3;
  int kso[12], c3o[3];
#pragma unroll
  for (int j = 0; j < 12; ++j) kso[j] = (j + rot) % 12;
#pragma unroll
  for (int j = 0; j < 3; ++j) c3o[j] = (j + rot2) % 3;
#define WOFF(s) ((((s) / 36) * 36 + kso[((s) % 36) / 3] * 3 + c3o[(s) % 3]) * 1024)
  bf16x8 w[K1_D];
#pragma unroll
  for (int s = 0; s < 3; ++s) w[s] = ld16(wp + WOFF(s));
  SB0();
  {
    if (!ok) { xr[0] = bf16x8{}; xr[1] = bf16x8{}; xr[2] = bf16x8{}; }
    bf16x8 h[3]; float m_, s_; ln_row(xr, h, m_, s_);
#pragma unroll
    for (int i = 0; i < 3; ++i) *(bf16x8*)(Ht + slot * ROWB + 16 * ((r16 + 16 * i) ^ swz(slot & 15))) = h[i];
    asm volatile("" : "+v"(xr[0]), "+v"(xr[1]), "+v"(xr[2]));
  }
  SB0();
#pragma unroll
  for (int s = 3; s < K1_D; ++s) w[s] = ld16(wp + WOFF(s));
  lds_barrier();
  f32x4 acc[3][2];
#define HFRAG(u, jk) (*(const bf16x8*)(Ht + (16 * (u) + r16) * ROWB + 16 * ((4 * kso[jk] + g) ^ swz(r16))))
#define K1_GEMM(MAT, TR, HOOK) do {                                                        \
    bf16x8 hb[2][2];                                                                        \
    hb[0][0] = HFRAG(0, 0); hb[0][1] = HFRAG(1, 0);                                         \
    _Pragma("unroll") for (int c = 0; c < 3; ++c) { acc[c][0] = f32x4{}; acc[c][1] = f32x4{}; } \
    _Pragma("unroll") for (int jk = 0; jk < 12; ++jk) {                                     \
      if (jk < 11) { hb[(jk + 1) & 1][0] = HFRAG(0, jk + 1); hb[(jk + 1) & 1][1] = HFRAG(1, jk + 1); } \
      HOOK;                                                                                 \
      _Pragma("unroll") for (int c = 0; c < 3; ++c) {                                       \
        const int s = (MAT) * 36 + jk * 3 + c;                                              \
        const bf16x8 wf = w[s % K1_D];                                                      \
        if (s + K1_D < 72) w[s % K1_D] = ld16(wp + WOFF(s + K1_D));                         \
        if (TR) { acc[c][0] = MFMA16(wf, hb[jk & 1][0], acc[c][0]); acc[c][1] = MFMA16(wf, hb[jk & 1][1], acc[c][1]); } \
        else    { acc[c][0] = MFMA16(hb[jk & 1][0], wf, acc[c][0]); acc[c][1] = MFMA16(hb[jk & 1][1], wf, acc[c][1]); } \
        SB0();                                                                              \
      }                                                                                     \
    }                                                                                       \
  } while (0)

  K1_GEMM(0, true, );
#pragma unroll
  for (int u = 0; u < 2; ++u) {
    bf16x4 hs[3];
#pragma unroll
    for (int c = 0; c < 3; ++c)
      hs[c] = *(const bf16x4*)(Ht + (16 * u + r16) * ROWB + 8 * (g & 1) + 16 * ((6 * wid + 2 * c3o[c] + (g >> 1)) ^ swz(r16)));
    const bf16x4 z = {};
    f32x4 sacc = MFMA16(cat8(hs[0], hs[1]), cat8(pack4(acc[0][u]), pack4(acc[1][u])), f32x4{});
    sacc = MFMA16(cat8(hs[2], z), cat8(pack4(acc[2][u]), z), sacc);
    *(f32x4*)(Sp + (wid * 2 + u) * 1024 + lane * 16) = sacc;
  }
  SB0();
  bf16x8 pf[2];
  auto tsoftmax = [&](int u) {
    const bool blk = (g >> 1) == (r16 >> 3);
    f32x4 sp[8];
#pragma unroll
    for (int ww = 0; ww < 8; ++ww) sp[ww] = *(const f32x4*)(Sp + (ww * 2 + u) * 1024 + lane * 16);
    f32x4 s = ((sp[0] + sp[1]) + (sp[2] + sp[3])) + ((sp[4] + sp[5]) + (sp[6] + sp[7]));
    if (g & 1) s[3] = NEG;
    const float mx = xmax16(fmaxf(fmaxf(s[0], s[1]), fmaxf(s[2], s[3])));
    f32x4 p;
#pragma unroll
    for (int r = 0; r < 4; ++r) p[r] = __builtin_amdgcn_exp2f(s[r] - mx);
    const float sum = xsum16((p[0] + p[1]) + (p[2] + p[3]));
    const float inv = blk ? __builtin_amdgcn_rcpf(sum) : 0.f;
    const bf16x4 pb = pack4(p * inv), z = {};
    pf[u] = u == 0 ? cat8(pb, z) : cat8(z, pb);
  };
  K1_GEMM(1, false, { if (jk == 5) lds_barrier(); if (jk == 6) tsoftmax(0); if (jk == 8) tsoftmax(1); });
  {
#pragma unroll
    for (int c = 0; c < 3; ++c) {
      const bf16x8 vf = cat8(pack4(acc[c][0]), pack4(acc[c][1]));
#pragma unroll
      for (int u = 0; u < 2; ++u)
        *(f32x4*)(Mx + (16 * u + r16) * MIXB + (48 * wid + 16 * c3o[c] + 4 * g) * 4) = MFMA16(vf, pf[u], f32x4{});
    }
  }
  lds_barrier();
  bf16x8 x1[3], h1[3];
  float mean1, std1;
  {
#pragma unroll
    for (int i = 0; i < 3; ++i) {
      f32x4 m0 = *(const f32x4*)(Mx + slot * MIXB + (r16 + 16 * i) * 32), m1 = *(const f32x4*)(Mx + slot * MIXB + (r16 + 16 * i) * 32 + 16);
#pragma unroll
      for (int j = 0; j < 4; ++j) { m0[j] += (float)xr[i][j]; m1[j] += (float)xr[i][4 + j]; }
      x1[i] = cat8(pack4(m0), pack4(m1));
    }
    ln_row(x1, h1, mean1, std1);
  }
  if (ok) {
    char* ho = ws + WS_H1 + (size_t)(t * NP + n) * ROWB;
#pragma unroll
    for (int i = 0; i < 3; ++i) __builtin_nontemporal_store(h1[i], (bf16x8*)(ho + 16 * ((r16 + 16 * i) ^ swz(n & 15))));
    if (r16 == 0) *(float2*)(ws + WS_ST + (size_t)(t * NP + n) * 8) = make_float2(mean1, std1);
  }
}

constexpr int SLOT = 24576;
constexpr int K2_P = 6 * SLOT;
constexpr int K2_MX = K2_P + 8192;
constexpr int K2_L = K2_MX + 512;
constexpr int K2_AL = K2_L + 512;
constexpr int K2_FL = K2_AL + 128;
constexpr int K2_LDS = K2_FL + 64;
constexpr float T_BIG = 1.8446744e19f;

DI void dma6(const char* src, int dst, int vo) {
  asm volatile("s_mov_b32 m0, %3\n\ts_nop 0\n\t"
               "global_load_lds_dwordx4 %0, %2\n\tglobal_load_lds_dwordx4 %0, %2 offset:1024\n\t"
               "global_load_lds_dwordx4 %0, %2 offset:2048\n\tglobal_load_lds_dwordx4 %0, %2 offset:3072\n\t"
               "s_mov_b32 m0, %4\n\ts_nop 0\n\t"
               "global_load_lds_dwordx4 %1, %2\n\tglobal_load_lds_dwordx4 %1, %2 offset:1024"
               :: "v"(vo), "v"(vo + 4096), "s"(src), "s"(dst), "s"(dst + 4096) : "memory");
}
#define VMWAIT(n) asm volatile("s_waitcnt vmcnt(" #n ")" ::: "memory")
DI float vmax(float a, float b) { return fmaxf(a, b); }
DI float vmax3(float a, float b, float c) { return fmaxf(fmaxf(a, b), c); }
DI float xm16(float v) { return xmax16(v); }
DI float xm32(float v) { return xmax32(v); }
#define RARE(x) __builtin_expect((x) != 0, 0)
#define LDSU(off) (*(volatile LDS_AS unsigned*)(sm + (off)))

#define FRESH_LANE() int lane_ = __builtin_amdgcn_mbcnt_hi(-1, __builtin_amdgcn_mbcnt_lo(-1, 0)); asm volatile("" : "+v"(lane_)); \
    const int lane = lane_, g = lane >> 4, r16 = lane & 15
__global__ __launch_bounds__(768, 3) void k2_spatial(const char* __restrict__ ws, char* __restrict__ out) {
  extern __shared__ __attribute__((aligned(16))) char sm[];
  const int f = blockIdx.x & 7, qt = blockIdx.x >> 3;
  if (f >= NT) return;
  const int tid = threadIdx.x, wid = __builtin_amdgcn_readfirstlane(tid >> 6), lane = tid & 63, g = lane >> 4, r16 = lane & 15;
  const int w4 = wid & 3;
  const char* wv = ws + WS_WVOS;
  bf16x8 wreg[24];
  const int rotw = qt % 12;
#define WFR(c, j) ld16(wv + wid * SLOT + ((c) * 12 + ((j) + rotw) % 12) * 1024 + lane * 16)
  const int dvo = lane * 16 + w4 * 6144, dq = w4 * 6144;

  if (wid >= 8) {
    const char* kv = ws + WS_H1 + (size_t)f * NP * ROWB;
#define KVH(k) (kv + ((qt + (k)) % 23) * SLOT)
#define BAR() do { __builtin_amdgcn_s_barrier(); asm volatile("" ::: "memory"); } while (0)
    if (w4 == 0 && lane < 16) LDSU(K2_FL + 4 * lane) = 0u;
    lds_barrier();
    dma6(KVH(0), dq, dvo); dma6(KVH(1), SLOT + dq, dvo);
    VMWAIT(0); BAR();
    dma6(KVH(2), 2 * SLOT + dq, dvo); dma6(KVH(3), 3 * SLOT + dq, dvo);
    BAR();
    dma6(KVH(4), 4 * SLOT + dq, dvo);
    VMWAIT(6);
#pragma unroll
    for (int i = 1; i <= 12; ++i) {
      BAR();
      const int fb = ((i + 2) % 3) * 2 * SLOT;
      const unsigned fl = LDSU(K2_FL + 4 * (i - 1));
      if (i == 1) { dma6(KVH(5), 5 * SLOT + dq, dvo); dma6(KVH(6), dq, dvo); dma6(KVH(7), SLOT + dq, dvo); }
      if (i >= 2 && i <= 11) {
        dma6(i <= 9 ? KVH(2 * i + 4) : wv + (2 * i - 16) * SLOT, fb + dq, dvo);
        if (i != 9) dma6(i <= 8 ? KVH(2 * i + 5) : wv + (2 * i - 15) * SLOT, fb + SLOT + dq, dvo);
      }
      if (i >= 2 && i <= 9) {
#pragma unroll
        for (int j = 3 * (i - 2); j < 3 * (i - 1); ++j) wreg[j] = WFR(j / 12, j % 12);
      }
      if (i >= 2) { if (RARE(__builtin_amdgcn_readfirstlane(fl))) { BAR(); BAR(); } }
      if (i == 1 || i == 10) VMWAIT(12); else if (i == 9) VMWAIT(9); else if (i < 9) VMWAIT(15);
    }
    VMWAIT(0);
  } else {
  {
    const char* wp = ws + WS_W1 + (size_t)(wid * 108 + 72) * 1024 + lane * 16;
#define QW(s) ld16(wp + ((((s) / 3 + rotw) % 12) * 3 + (s) % 3) * 1024)
#define QH(u, jk) (*(const bf16x8*)(sm + 4 * SLOT + (16 * (u) + r16) * ROWB + 16 * ((4 * (((jk) + rotw) % 12) + g) ^ swz(r16))))
    bf16x8 w[12], hb[2][2];
    f32x4 acc[3][2] = {};
    asm volatile("s_mov_b32 m0, %2\n\ts_nop 0\n\tglobal_load_lds_dwordx4 %0, %1\n\tglobal_load_lds_dwordx4 %0, %1 offset:1024\n\t"
                 "global_load_lds_dwordx4 %0, %1 offset:2048"
                 :: "v"(lane * 16 + wid * 3072), "s"(ws + WS_H1 + (size_t)(f * NP + 32 * qt) * ROWB), "s"(4 * SLOT + wid * 3072) : "memory");
    lds_barrier();
#pragma unroll
    for (int s = 0; s < 6; ++s) w[s] = QW(s);
    VMWAIT(6);
    if (lane == 0) __hip_atomic_fetch_add((LDS_AS unsigned*)(sm + K2_FL + 60), 1u, __ATOMIC_RELAXED, __HIP_MEMORY_SCOPE_WORKGROUP);
#pragma unroll
    for (int s = 6; s < 12; ++s) w[s] = QW(s);
    while (__builtin_amdgcn_readfirstlane(LDSU(K2_FL + 60)) < 8u) __builtin_amdgcn_s_sleep(1);
    asm volatile("" ::: "memory");
    hb[0][0] = QH(0, 0); hb[0][1] = QH(1, 0);
#pragma unroll
    for (int jk = 0; jk < 12; ++jk) {
      if (jk < 11) { hb[(jk + 1) & 1][0] = QH(0, jk + 1); hb[(jk + 1) & 1][1] = QH(1, jk + 1); }
#pragma unroll
      for (int c = 0; c < 3; ++c) {
        const int s = jk * 3 + c;
        const bf16x8 wf = w[s % 12];
        if (s + 12 < 36) w[s % 12] = QW(s + 12);
        acc[c][0] = MFMA16(wf, hb[jk & 1][0], acc[c][0]); acc[c][1] = MFMA16(wf, hb[jk & 1][1], acc[c][1]);
        SB0();
      }
    }
#pragma unroll
    for (int c = 0; c < 3; ++c) {
      char* qp = sm + 5 * SLOT + r16 * ROWB + 16 * ((6 * wid + 2 * c + (g >> 1)) ^ swz(r16)) + 8 * (g & 1);
      *(bf16x4*)qp = pack4(acc[c][0]); *(bf16x4*)(qp + 16 * ROWB) = pack4(acc[c][1]);
    }
  }
  if (wid < 4) {
    __builtin_amdgcn_s_setprio(2);
    FRESH_LANE();
    const int fsw = swz(r16);
    int roff[2][4];
#pragma unroll
    for (int b = 0; b < 4; ++b) { roff[0][b] = (16 * w4 + r16) * ROWB + 16 * ((4 * b + g) ^ fsw); roff[1][b] = roff[0][b] + 4 * SLOT; }
#define KFRAG(tbuf, ks) (*(const bf16x8*)(sm + roff[(tbuf) >> 1][(ks) & 3] + ((tbuf) & 1) * 2 * SLOT + ((ks) >> 2) * 256))
    const int pw_off = K2_P + r16 * 128 + 16 * ((2 * w4 + (g >> 1)) ^ (r16 & 7)) + 8 * (g & 1);
    const int kpad = (w4 & 1) ? 22 - qt : -1;
    bf16x8 qf[2][12];
    float R[2], l[2] = {0.f, 0.f}, ls[2];
    f32x4 s[2];
    auto mask_u = [&](int i, int u) {
      const int kidx = 2 * i + (w4 >> 1);
      if (RARE(kidx >= 23 || kidx == kpad)) {
#pragma unroll
        for (int r = 0; r < 4; ++r) s[u][r] = (kidx >= 23 || 4 * g + r >= 9) ? NEG : s[u][r];
      }
    };
    auto put_p = [&](int i) {
      f32x4 p[2];
#pragma unroll
      for (int r = 0; r < 4; ++r) { p[0][r] = __builtin_amdgcn_exp2f(s[0][r]); p[1][r] = __builtin_amdgcn_exp2f(s[1][r]); }
      *(bf16x4*)(sm + (i & 1) * 4096 + pw_off) = pack4(p[0]); *(bf16x4*)(sm + (i & 1) * 4096 + 2048 + pw_off) = pack4(p[1]);
      ls[0] = (p[0][0] + p[0][1]) + (p[0][2] + p[0][3]); ls[1] = (p[1][0] + p[1][1]) + (p[1][2] + p[1][3]);
    };
    auto put_max = [&]() {
      const float m0 = xm32(xm16(vmax(vmax3(s[0][0], s[0][1], s[0][2]), s[0][3]))), m1 = xm32(xm16(vmax(vmax3(s[1][0], s[1][1], s[1][2]), s[1][3])));
      if (g == 0) { *(float*)(sm + K2_MX + r16 * 16 + w4 * 4) = m0; *(float*)(sm + K2_MX + (16 + r16) * 16 + w4 * 4) = m1; }
    };
    auto get_max = [&](float (&M)[2]) {
#pragma unroll
      for (int u = 0; u < 2; ++u) {
        const f32x4 mm = *(const f32x4*)(sm + K2_MX + (16 * u + r16) * 16);
        M[u] = vmax(vmax3(mm[0], mm[1], mm[2]), mm[3]);
      }
    };
    auto redo = [&](int i) {
      put_max();
      lds_barrier();
      float M[2], a[2]; get_max(M);
#pragma unroll
      for (int u = 0; u < 2; ++u) { const float d = vmax(M[u], 0.f); a[u] = __builtin_amdgcn_exp2f(-d); R[u] += d; l[u] *= a[u]; s[u] -= d; }
      put_p(i - 1);
      if (wid == 0 && g == 0) { *(float*)(sm + K2_AL + r16 * 4) = a[0]; *(float*)(sm + K2_AL + 64 + r16 * 4) = a[1]; }
      lds_barrier();
    };

    lds_barrier();
    {
      bf16x8 kf[12];
#pragma unroll
      for (int ks = 0; ks < 12; ++ks) {
        kf[ks] = *(const bf16x8*)(sm + roff[0][ks & 3] + (ks >> 2) * 256);
        qf[0][ks] = *(const bf16x8*)(sm + roff[1][ks & 3] - 16 * w4 * ROWB + SLOT + (ks >> 2) * 256);
        qf[1][ks] = *(const bf16x8*)(sm + roff[1][ks & 3] - 16 * w4 * ROWB + SLOT + 16 * ROWB + (ks >> 2) * 256);
      }
      s[0] = f32x4{}; s[1] = f32x4{};
#pragma unroll
      for (int ks = 0; ks < 12; ++ks) { s[0] = MFMA16(kf[ks], qf[0][ks], s[0]); s[1] = MFMA16(kf[ks], qf[1][ks], s[1]); }
      mask_u(0, 0); mask_u(0, 1);
      put_max();
    }
    lds_barrier();
    get_max(R);
    s[0] -= R[0]; s[1] -= R[1];
    put_p(0);
#pragma unroll
    for (int i = 1; i <= 11; ++i) {
      lds_barrier();
      bf16x8 kf[6];
      const unsigned fl = LDSU(K2_FL + 4 * (i - 1));
#pragma unroll
      for (int ks = 0; ks < 6; ++ks) kf[ks] = KFRAG(i % 3, ks);
      if (i > 1) { if (RARE(__builtin_amdgcn_readfirstlane(fl))) redo(i); }
      l[0] += ls[0]; l[1] += ls[1];
      SB0();
      s[0] = f32x4{-R[0], -R[0], -R[0], -R[0]}; s[1] = f32x4{-R[1], -R[1], -R[1], -R[1]};
#pragma unroll
      for (int ks = 0; ks < 12; ++ks) {
        s[0] = MFMA16(kf[ks % 6], qf[0][ks], s[0]);
        s[1] = MFMA16(kf[ks % 6], qf[1][ks], s[1]);
        if (ks < 6) kf[ks] = KFRAG(i % 3, 6 + ks);
        if (i == 11) { wreg[ks] = WFR(0, ks); wreg[12 + ks] = WFR(1, ks); }
        SB0();
      }
      mask_u(i, 0); mask_u(i, 1);
      put_p(i);
      if (RARE(__any(!(ls[0] + ls[1] < T_BIG)))) { if (lane == 0) LDSU(K2_FL + 4 * i) = 1u; }
    }
    lds_barrier();
    if (RARE(__builtin_amdgcn_readfirstlane(LDSU(K2_FL + 4 * 11)))) redo(12);
#pragma unroll
    for (int u = 0; u < 2; ++u) {
      const float t = xsum32(xsum16(l[u] + ls[u]));
      if (g == 0) *(float*)(sm + K2_L + (16 * u + r16) * 16 + w4 * 4) = t;
    }
    __builtin_amdgcn_s_setprio(0);
  } else {
    FRESH_LANE();
    const int vsw = (8 * (g & 1)) | ((r16 >> 2) << 1);
    int voff[2][6];
#pragma unroll
    for (int i = 0; i < 6; ++i) {
      voff[0][i] = (8 * g + (r16 >> 2)) * ROWB + 16 * ((12 * w4 + 2 * i + ((lane & 3) >> 1)) ^ vsw) + 8 * (lane & 1);
      voff[1][i] = voff[0][i] + 3 * SLOT;
    }
    int pr_off[2];
#pragma unroll
    for (int hh = 0; hh < 2; ++hh) pr_off[hh] = K2_P + r16 * 128 + 16 * ((4 * hh + g) ^ (r16 & 7));
    f32x4 o[6][2];
#pragma unroll
    for (int i = 0; i < 6; ++i) { o[i][0] = f32x4{}; o[i][1] = f32x4{}; }
    bf16x8 vf[2][6];
    auto ld_v = [&](int tbuf, int hh, int c) {
      const int sl = 2 * tbuf + hh;
      return cat8(tr_read(sm + voff[sl / 3][c] + (sl % 3) * SLOT), tr_read(sm + voff[sl / 3][c] + (sl % 3) * SLOT + 4 * ROWB));
    };
    auto redo_wait = [&]() {
      lds_barrier(); lds_barrier();
      const float a0 = *(const float*)(sm + K2_AL + r16 * 4), a1 = *(const float*)(sm + K2_AL + 64 + r16 * 4);
#pragma unroll
      for (int c = 0; c < 6; ++c) { o[c][0] *= a0; o[c][1] *= a1; }
    };
    lds_barrier();
#pragma unroll
    for (int c = 0; c < 6; ++c) { vf[0][c] = ld_v(0, 0, c); vf[1][c] = ld_v(0, 1, c); }
    lds_barrier();
#pragma unroll
    for (int i = 1; i <= 11; ++i) {
      lds_barrier();
      const int sp = ((i - 1) & 1) * 4096;
      const unsigned fl = LDSU(K2_FL + 4 * (i - 1));
      bf16x8 pf[2][2];
#pragma unroll
      for (int hh = 0; hh < 2; ++hh) { pf[hh][0] = *(const bf16x8*)(sm + sp + pr_off[hh]); pf[hh][1] = *(const bf16x8*)(sm + sp + pr_off[hh] + 2048); }
      if (i > 1) {
        if (RARE(__builtin_amdgcn_readfirstlane(fl))) {
          redo_wait();
#pragma unroll
          for (int hh = 0; hh < 2; ++hh) { pf[hh][0] = *(const bf16x8*)(sm + sp + pr_off[hh]); pf[hh][1] = *(const bf16x8*)(sm + sp + pr_off[hh] + 2048); }
        }
      }
      SB0();
#pragma unroll
      for (int hh = 0; hh < 2; ++hh)
#pragma unroll
        for (int c = 0; c < 6; ++c) {
          o[c][0] = MFMA16(vf[hh][c], pf[hh][0], o[c][0]);
          o[c][1] = MFMA16(vf[hh][c], pf[hh][1], o[c][1]);
          if (i < 11 || hh == 0) vf[hh][c] = ld_v(i % 3, hh, c);
          SB0();
        }
    }
    lds_barrier();
    if (RARE(__builtin_amdgcn_readfirstlane(LDSU(K2_FL + 4 * 11)))) redo_wait();
    {
      const bf16x8 p0 = *(const bf16x8*)(sm + 4096 + pr_off[0]), p1 = *(const bf16x8*)(sm + 4096 + pr_off[0] + 2048);
#pragma unroll
      for (int c = 0; c < 6; ++c) { o[c][0] = MFMA16(vf[0][c], p0, o[c][0]); o[c][1] = MFMA16(vf[0][c], p1, o[c][1]); }
    }
    int lanee = __builtin_amdgcn_mbcnt_hi(-1, __builtin_amdgcn_mbcnt_lo(-1, 0));
    asm volatile("" : "+v"(lanee));
    const int ge = lanee >> 4, r16e = lanee & 15;
#pragma unroll
    for (int c = 0; c < 6; ++c) {
      char* op = sm + 5 * SLOT + r16e * ROWB + 16 * ((12 * w4 + 2 * c + (ge >> 1)) ^ swz(r16e)) + 8 * (ge & 1);
      *(bf16x4*)op = pack4(o[c][0]); *(bf16x4*)(op + 16 * ROWB) = pack4(o[c][1]);
    }
  }
  }

  int lanet = __builtin_amdgcn_mbcnt_hi(-1, __builtin_amdgcn_mbcnt_lo(-1, 0));
  asm volatile("" : "+v"(lanet));
  const int gt = lanet >> 4, r16t = lanet & 15;
  bf16x4 xres[2][2];
  float2 st[2];
#pragma unroll
  for (int u = 0; u < 2; ++u) {
    const int q = 32 * qt + 16 * u + r16t;
    const char* xp = ws + WS_H1 + (size_t)(f * NP + q) * ROWB + 8 * (gt & 1);
    xres[0][u] = *(const bf16x4*)(xp + 16 * ((4 * wid + (gt >> 1)) ^ swz(r16t))); xres[1][u] = *(const bf16x4*)(xp + 16 * ((4 * wid + 2 + (gt >> 1)) ^ swz(r16t)));
    st[u] = *(const float2*)(ws + WS_ST + (size_t)(f * NP + (q < NN ? q : 0)) * 8);
  }
  int ooff[4];
#pragma unroll
  for (int b = 0; b < 4; ++b) ooff[b] = 5 * SLOT + r16t * ROWB + 16 * ((4 * b + gt) ^ swz(r16t));
  lds_barrier();
  f32x4 acc[2][2] = {};
  float inv[2];
#pragma unroll
  for (int u = 0; u < 2; ++u) {
    const f32x4 ll = *(const f32x4*)(sm + K2_L + (16 * u + r16t) * 16);
    inv[u] = __builtin_amdgcn_rcpf((ll[0] + ll[1]) + (ll[2] + ll[3]));
  }
#define OFRAG(u, ks) (*(const bf16x8*)(sm + ooff[(ks) & 3] + (u) * 16 * ROWB + ((ks) >> 2) * 256))
  if (wid >= 4 && wid < 8) {
    const char* wl = sm + w4 * SLOT + lanet * 16;
#pragma unroll
    for (int ks = 0; ks < 12; ++ks) {
      const bf16x8 o0 = OFRAG(0, ks), o1 = OFRAG(1, ks), w0 = *(const bf16x8*)(wl + ks * 1024), w1 = *(const bf16x8*)(wl + (12 + ks) * 1024);
      acc[0][0] = MFMA16(w0, o0, acc[0][0]); acc[0][1] = MFMA16(w0, o1, acc[0][1]);
      acc[1][0] = MFMA16(w1, o0, acc[1][0]); acc[1][1] = MFMA16(w1, o1, acc[1][1]);
    }
  } else {
#pragma unroll
    for (int ks = 0; ks < 12; ++ks) {
      const char* ob = sm + 5 * SLOT + r16t * ROWB + 16 * ((4 * ((ks + rotw) % 12) + gt) ^ swz(r16t));
      const bf16x8 o0 = *(const bf16x8*)ob, o1 = *(const bf16x8*)(ob + 16 * ROWB);
      acc[0][0] = MFMA16(wreg[ks], o0, acc[0][0]); acc[0][1] = MFMA16(wreg[ks], o1, acc[0][1]);
      acc[1][0] = MFMA16(wreg[12 + ks], o0, acc[1][0]); acc[1][1] = MFMA16(wreg[12 + ks], o1, acc[1][1]);
    }
  }
#pragma unroll
  for (int u = 0; u < 2; ++u) {
    const int q = 32 * qt + 16 * u + r16t;
    if (q < NN) {
      char* op = out + (size_t)(f * NN + q) * ROWB + (32 * wid + 4 * gt) * 2;
#pragma unroll
      for (int c = 0; c < 2; ++c) {
        f32x4 v = acc[c][u];
#pragma unroll
        for (int r = 0; r < 4; ++r) v[r] = v[r] * inv[u] + ((float)xres[c][u][r] * st[u].y + st[u].x);
        *(bf16x4*)(op + c * 32) = pack4(v);
      }
    }
  }
}

}

extern "C" void kernel_launch(void* const* d_in, const int* in_sizes, int n_in,
                              void* d_out, int out_size, void* d_ws, size_t ws_size,
                              hipStream_t stream) {
  (void)in_sizes; (void)n_in; (void)out_size; (void)ws_size;
  const char* x = (const char*)d_in[0];
  char* ws = (char*)d_ws;
  (void)hipFuncSetAttribute((const void*)k2_spatial, hipFuncAttributeMaxDynamicSharedMemorySize, K2_LDS);
  k0_fold<<<256, 256, 0, stream>>>((const char*)d_in[1], (const char*)d_in[2], (const char*)d_in[3], (const char*)d_in[4], ws, x, (const char*)d_out);
  k1_temporal<<<183, 512, 0, stream>>>(x, ws);
  k2_spatial<<<184, 768, K2_LDS, stream>>>(ws, (char*)d_out);
}
```

```cpp
#include <hip/hip_runtime.h>
#include <hip/hip_bf16.h>
#include <stdint.h>

typedef __attribute__((ext_vector_type(8))) __bf16 bf16x8;
typedef __attribute__((ext_vector_type(4))) __bf16 bf16x4;
typedef __attribute__((ext_vector_type(4))) float f32x4;
typedef __attribute__((ext_vector_type(4))) unsigned u32x4;
#define LDS_AS __attribute__((address_space(3)))
#define DI __device__ __forceinline__
#define MFMA16(a, b, c) __builtin_amdgcn_mfma_f32_16x16x32_bf16((a), (b), (c), 0, 0, 0)

namespace {

constexpr int NT = 7, NN = 729, NP = 736;
constexpr int ROWB = 768;
constexpr size_t WS_W1   = 0;
constexpr size_t WS_WVOS = 884736;
constexpr size_t WS_ST   = WS_WVOS + 294912;
constexpr size_t WS_H1   = WS_ST + 45056;
constexpr float QSCALE = 0.05103103630798288f * 1.4426950408889634f;
constexpr float NEG = -1e30f;

DI int swz(int k) { return (k & 8) | ((k & 3) << 1); }
DI void lds_barrier() { asm volatile("s_waitcnt lgkmcnt(0)" ::: "memory"); __builtin_amdgcn_s_barrier(); asm volatile("" ::: "memory"); }
DI bf16x8 ld16(const void* p) { return *(const bf16x8*)p; }
DI bf16x4 pack4(f32x4 v) { bf16x4 r = {(__bf16)v[0], (__bf16)v[1], (__bf16)v[2], (__bf16)v[3]}; return r; }
DI bf16x8 cat8(bf16x4 a, bf16x4 b) { return __builtin_shufflevector(a, b, 0, 1, 2, 3, 4, 5, 6, 7); }
DI bf16x4 tr_read(const char* p) { return __builtin_amdgcn_ds_read_tr16_b64_v4bf16((LDS_AS bf16x4*)p); }
template <int CTRL> DI float dpp_mov(float v) { return __int_as_float(__builtin_amdgcn_update_dpp(0, __float_as_int(v), CTRL, 0xf, 0xf, false)); }
DI float red16_sum(float v) {
  v += dpp_mov<0x128>(v); v += dpp_mov<0x124>(v); v += dpp_mov<0x122>(v); v += dpp_mov<0x121>(v); return v;
}
DI float xmax16(float v) { auto r = __builtin_amdgcn_permlane16_swap(__float_as_uint(v), __float_as_uint(v), false, false); return fmaxf(__uint_as_float(r[0]), __uint_as_float(r[1])); }
DI float xmax32(float v) { auto r = __builtin_amdgcn_permlane32_swap(__float_as_uint(v), __float_as_uint(v), false, false); return fmaxf(__uint_as_float(r[0]), __uint_as_float(r[1])); }
DI float xsum16(float v) { auto r = __builtin_amdgcn_permlane16_swap(__float_as_uint(v), __float_as_uint(v), false, false); return __uint_as_float(r[0]) + __uint_as_float(r[1]); }
DI float xsum32(float v) { auto r = __builtin_amdgcn_permlane32_swap(__float_as_uint(v), __float_as_uint(v), false, false); return __uint_as_float(r[0]) + __uint_as_float(r[1]); }
#define SB0() __builtin_amdgcn_sched_barrier(0)

DI void ln_row(const bf16x8 (&xin)[3], bf16x8 (&hout)[3], float& mean_o, float& std_o) {
  f32x4 v[6];
#pragma unroll
  for (int i = 0; i < 3; ++i)
#pragma unroll
    for (int j = 0; j < 4; ++j) { v[2 * i][j] = (float)xin[i][j]; v[2 * i + 1][j] = (float)xin[i][4 + j]; }
  const f32x4 s4 = ((v[0] + v[1]) + (v[2] + v[3])) + (v[4] + v[5]);
  const float mean = red16_sum((s4[0] + s4[1]) + (s4[2] + s4[3])) * (1.f / 384.f);
  f32x4 q4 = {};
#pragma unroll
  for (int k = 0; k < 6; ++k) { v[k] -= mean; q4 += v[k] * v[k]; }
  const float ve = red16_sum((q4[0] + q4[1]) + (q4[2] + q4[3])) * (1.f / 384.f) + 1e-5f, rstd = rsqrtf(ve);
  mean_o = mean; std_o = ve * rstd;
#pragma unroll
  for (int i = 0; i < 3; ++i) hout[i] = cat8(pack4(v[2 * i] * rstd), pack4(v[2 * i + 1] * rstd));
}

__global__ __launch_bounds__(256) void k0_fold(const char* __restrict__ wqkv_t, const char* __restrict__ wo_t,
                                               const char* __restrict__ wqkv_s, const char* __restrict__ wo_s,
                                               char* __restrict__ ws) {
  __shared__ __attribute__((aligned(16))) char sm[384 * 112];
  const int b = blockIdx.x, mat = (b & 7) >> 1, ti = 4 * (b & 1) + (b >> 6), tj = (b >> 3) & 7;
  const int tid = threadIdx.x, wid = __builtin_amdgcn_readfirstlane(tid >> 6), lane = tid & 63, g = lane >> 4, r16 = lane & 15;
  const char* wqkv = mat < 2 ? wqkv_t : wqkv_s;
  const char* wo = mat < 2 ? wo_t : wo_s;
  const bool isqk = (mat & 1) == 0;
  bf16x8 a[12];
  if (wid < 3) {
    const char* ap = wqkv + ((48 * ti + 16 * wid + r16) * 1152 + (isqk ? 0 : 768) + 8 * g) * 2;
#pragma unroll
    for (int ks = 0; ks < 12; ++ks) a[ks] = ld16(ap + ks * 64);
  }
  if (isqk) {
#pragma unroll
    for (int it = 0; it < 9; ++it) {
      const int idx = tid + 256 * it, row = idx / 48, ch = idx % 48;
      *(bf16x8*)(sm + row * 768 + 16 * (ch ^ swz(row & 15))) = ld16(wqkv + ((48 * tj + row) * 1152 + 384 + ch * 8) * 2);
    }
  } else {
#pragma unroll
    for (int it = 0; it < 9; ++it) {
      const int idx = tid + 256 * it, row = idx / 6, ch = idx % 6;
      *(bf16x8*)(sm + row * 112 + ch * 16) = ld16(wo + (row * 384 + 48 * tj + ch * 8) * 2);
    }
  }
  if (wid == 3 && b < 7) {
    char* dst = ws + WS_H1 + ((size_t)b * NP + NN) * ROWB;
    const u32x4 z = {0u, 0u, 0u, 0u};
    for (int i = lane; i < 7 * 48; i += 64) *(u32x4*)(dst + i * 16) = z;
  }
  __syncthreads();
  if (wid < 3) {
    f32x4 acc[3] = {};
    if (isqk) {
#pragma unroll
      for (int ks = 0; ks < 12; ++ks)
#pragma unroll
        for (int jt = 0; jt < 3; ++jt)
          acc[jt] = MFMA16(a[ks], *(const bf16x8*)(sm + (16 * jt + r16) * 768 + 16 * ((4 * ks + g) ^ swz(r16))), acc[jt]);
    } else {
      const char* tb = sm + (8 * g + (r16 >> 2)) * 112 + (lane & 3) * 8;
#pragma unroll
      for (int ks = 0; ks < 12; ++ks)
#pragma unroll
        for (int jt = 0; jt < 3; ++jt) {
          const bf16x4 lo = tr_read(tb + (32 * ks) * 112 + jt * 32), hi = tr_read(tb + (32 * ks + 4) * 112 + jt * 32);
          acc[jt] = MFMA16(a[ks], cat8(lo, hi), acc[jt]);
        }
    }
    const float sc = isqk ? QSCALE : 1.f;
    const int IT = 3 * ti + wid, ks = IT >> 1, gp = 2 * (IT & 1) + (g >> 1);
#pragma unroll
    for (int jt = 0; jt < 3; ++jt) {
      const int JT = 3 * tj + jt;
      size_t off;
      if (mat == 3) off = WS_WVOS + (size_t)(JT * 12 + ks) * 1024;
      else off = WS_W1 + (size_t)(tj * 108 + mat * 36 + ks * 3 + jt) * 1024;
      *(bf16x4*)(ws + off + (r16 + 16 * gp) * 16 + 8 * (g & 1)) = pack4(acc[jt] * sc);
    }
  }
}

constexpr int K1_D = 12;
constexpr int MIXB = 1552;
__global__ __launch_bounds__(512, 2) void k1_temporal(const char* __restrict__ x, char* __restrict__ ws) {
  __shared__ __attribute__((aligned(16))) char sm[24576 + 32 * MIXB + 16384];
  char* const Ht = sm; char* const Mx = sm + 24576; char* const Sp = sm + 24576 + 32 * MIXB;
  const int tid = threadIdx.x, wid = __builtin_amdgcn_readfirstlane(tid >> 6), lane = tid & 63, g = lane >> 4, r16 = lane & 15;
  const int n0 = blockIdx.x * 4, n = n0 + (wid >> 1);
  const int slot = 4 * wid + g, t = slot & 7;
  const bool ok = (n < NN) && (t < NT);
  bf16x8 xr[3];
  {
    const char* xp = x + ((size_t)(ok ? t * NN + n : 0)) * ROWB + r16 * 16;
#pragma unroll
    for (int i = 0; i < 3; ++i) xr[i] = ld16(xp + i * 256);
  }
  SB0();
  const char* wp = ws + WS_W1 + (size_t)wid * 108 * 1024 + lane * 16;
  const int rot = (blockIdx.x >> 3) % 12, rot2 = ((blockIdx.x >> 3) / 12 + blockIdx.x) % 3;
  int kso[12], c3o[3];
#pragma unroll
  for (int j = 0; j < 12; ++j) kso[j] = (j + rot) % 12;
#pragma unroll
  for (int j = 0; j < 3; ++j) c3o[j] = (j + rot2) % 3;
#define WOFF(s) ((((s) / 36) * 36 + kso[((s) % 36) / 3] * 3 + c3o[(s) % 3]) * 1024)
  bf16x8 w[K1_D];
#pragma unroll
  for (int s = 0; s < 3; ++s) w[s] = ld16(wp + WOFF(s));
  SB0();
  {
    if (!ok) { xr[0] = bf16x8{}; xr[1] = bf16x8{}; xr[2] = bf16x8{}; }
    bf16x8 h[3]; float m_, s_; ln_row(xr, h, m_, s_);
#pragma unroll
    for (int i = 0; i < 3; ++i) *(bf16x8*)(Ht + slot * ROWB + 16 * ((r16 + 16 * i) ^ swz(slot & 15))) = h[i];
    asm volatile("" : "+v"(xr[0]), "+v"(xr[1]), "+v"(xr[2]));
  }
  SB0();
#pragma unroll
  for (int s = 3; s < K1_D; ++s) w[s] = ld16(wp + WOFF(s));
  lds_barrier();
  f32x4 acc[3][2];
#define HFRAG(u, jk) (*(const bf16x8*)(Ht + (16 * (u) + r16) * ROWB + 16 * ((4 * kso[jk] + g) ^ swz(r16))))
#define K1_GEMM(MAT, TR, HOOK) do {                                                        \
    bf16x8 hb[2][2];                                                                        \
    hb[0][0] = HFRAG(0, 0); hb[0][1] = HFRAG(1, 0);                                         \
    _Pragma("unroll") for (int c = 0; c < 3; ++c) { acc[c][0] = f32x4{}; acc[c][1] = f32x4{}; } \
    _Pragma("unroll") for (int jk = 0; jk < 12; ++jk) {                                     \
      if (jk < 11) { hb[(jk + 1) & 1][0] = HFRAG(0, jk + 1); hb[(jk + 1) & 1][1] = HFRAG(1, jk + 1); } \
      HOOK;                                                                                 \
      _Pragma("unroll") for (int c = 0; c < 3; ++c) {                                       \
        const int s = (MAT) * 36 + jk * 3 + c;                                              \
        const bf16x8 wf = w[s % K1_D];                                                      \
        if (s + K1_D < 72) w[s % K1_D] = ld16(wp + WOFF(s + K1_D));                         \
        if (TR) { acc[c][0] = MFMA16(wf, hb[jk & 1][0], acc[c][0]); acc[c][1] = MFMA16(wf, hb[jk & 1][1], acc[c][1]); } \
        else    { acc[c][0] = MFMA16(hb[jk & 1][0], wf, acc[c][0]); acc[c][1] = MFMA16(hb[jk & 1][1], wf, acc[c][1]); } \
        SB0();                                                                              \
      }                                                                                     \
    }                                                                                       \
  } while (0)

  K1_GEMM(0, true, );
#pragma unroll
  for (int u = 0; u < 2; ++u) {
    bf16x4 hs[3];
#pragma unroll
    for (int c = 0; c < 3; ++c)
      hs[c] = *(const bf16x4*)(Ht + (16 * u + r16) * ROWB + 8 * (g & 1) + 16 * ((6 * wid + 2 * c3o[c] + (g >> 1)) ^ swz(r16)));
    const bf16x4 z = {};
    f32x4 sacc = MFMA16(cat8(hs[0], hs[1]), cat8(pack4(acc[0][u]), pack4(acc[1][u])), f32x4{});
    sacc = MFMA16(cat8(hs[2], z), cat8(pack4(acc[2][u]), z), sacc);
    *(f32x4*)(Sp + (wid * 2 + u) * 1024 + lane * 16) = sacc;
  }
  SB0();
  bf16x8 pf[2];
  auto tsoftmax = [&](int u) {
    const bool blk = (g >> 1) == (r16 >> 3);
    f32x4 sp[8];
#pragma unroll
    for (int ww = 0; ww < 8; ++ww) sp[ww] = *(const f32x4*)(Sp + (ww * 2 + u) * 1024 + lane * 16);
    f32x4 s = ((sp[0] + sp[1]) + (sp[2] + sp[3])) + ((sp[4] + sp[5]) + (sp[6] + sp[7]));
    if (g & 1) s[3] = NEG;
    const float mx = xmax16(fmaxf(fmaxf(s[0], s[1]), fmaxf(s[2], s[3])));
    f32x4 p;
#pragma unroll
    for (int r = 0; r < 4; ++r) p[r] = __builtin_amdgcn_exp2f(s[r] - mx);
    const float sum = xsum16((p[0] + p[1]) + (p[2] + p[3]));
    const float inv = blk ? __builtin_amdgcn_rcpf(sum) : 0.f;
    const bf16x4 pb = pack4(p * inv), z = {};
    pf[u] = u == 0 ? cat8(pb, z) : cat8(z, pb);
  };
  K1_GEMM(1, false, { if (jk == 5) lds_barrier(); if (jk == 6) tsoftmax(0); if (jk == 8) tsoftmax(1); });
  {
#pragma unroll
    for (int c = 0; c < 3; ++c) {
      const bf16x8 vf = cat8(pack4(acc[c][0]), pack4(acc[c][1]));
#pragma unroll
      for (int u = 0; u < 2; ++u)
        *(f32x4*)(Mx + (16 * u + r16) * MIXB + (48 * wid + 16 * c3o[c] + 4 * g) * 4) = MFMA16(vf, pf[u], f32x4{});
    }
  }
  lds_barrier();
  bf16x8 x1[3], h1[3];
  float mean1, std1;
  {
#pragma unroll
    for (int i = 0; i < 3; ++i) {
      f32x4 m0 = *(const f32x4*)(Mx + slot * MIXB + (r16 + 16 * i) * 32), m1 = *(const f32x4*)(Mx + slot * MIXB + (r16 + 16 * i) * 32 + 16);
#pragma unroll
      for (int j = 0; j < 4; ++j) { m0[j] += (float)xr[i][j]; m1[j] += (float)xr[i][4 + j]; }
      x1[i] = cat8(pack4(m0), pack4(m1));
    }
    ln_row(x1, h1, mean1, std1);
  }
  if (ok) {
    char* ho = ws + WS_H1 + (size_t)(t * NP + n) * ROWB;
#pragma unroll
    for (int i = 0; i < 3; ++i) __builtin_nontemporal_store(h1[i], (bf16x8*)(ho + 16 * ((r16 + 16 * i) ^ swz(n & 15))));
    if (r16 == 0) *(float2*)(ws + WS_ST + (size_t)(t * NP + n) * 8) = make_float2(mean1, std1);
  }
}

constexpr int SLOT = 24576;
constexpr int K2_P = 6 * SLOT;
constexpr int K2_MX = K2_P + 8192;
constexpr int K2_L = K2_MX + 512;
constexpr int K2_AL = K2_L + 512;
constexpr int K2_FL = K2_AL + 128;
constexpr int K2_LDS = K2_FL + 64;
constexpr float T_BIG = 1.8446744e19f;

DI void dma6(const char* src, int dst, int vo) {
  asm volatile("s_mov_b32 m0, %3\n\ts_nop 0\n\t"
               "global_load_lds_dwordx4 %0, %2\n\tglobal_load_lds_dwordx4 %0, %2 offset:1024\n\t"
               "global_load_lds_dwordx4 %0, %2 offset:2048\n\tglobal_load_lds_dwordx4 %0, %2 offset:3072\n\t"
               "s_mov_b32 m0, %4\n\ts_nop 0\n\t"
               "global_load_lds_dwordx4 %1, %2\n\tglobal_load_lds_dwordx4 %1, %2 offset:1024"
               :: "v"(vo), "v"(vo + 4096), "s"(src), "s"(dst), "s"(dst + 4096) : "memory");
}
#define VMWAIT(n) asm volatile("s_waitcnt vmcnt(" #n ")" ::: "memory")
DI float vmax(float a, float b) { return fmaxf(a, b); }
DI float vmax3(float a, float b, float c) { return fmaxf(fmaxf(a, b), c); }
DI float xm16(float v) { return xmax16(v); }
DI float xm32(float v) { return xmax32(v); }
#define RARE(x) __builtin_expect((x) != 0, 0)
#define LDSU(off) (*(volatile LDS_AS unsigned*)(sm + (off)))

#define FRESH_LANE() int lane_ = __builtin_amdgcn_mbcnt_hi(-1, __builtin_amdgcn_mbcnt_lo(-1, 0)); asm volatile("" : "+v"(lane_)); \
    const int lane = lane_, g = lane >> 4, r16 = lane & 15
__global__ __launch_bounds__(768, 3) void k2_spatial(const char* __restrict__ ws, char* __restrict__ out) {
  extern __shared__ __attribute__((aligned(16))) char sm[];
  const int f = blockIdx.x & 7, qt = blockIdx.x >> 3;
  if (f >= NT) return;
  const int tid = threadIdx.x, wid = __builtin_amdgcn_readfirstlane(tid >> 6), lane = tid & 63, g = lane >> 4, r16 = lane & 15;
  const int w4 = wid & 3;
  const char* wv = ws + WS_WVOS;
  bf16x8 wreg[24];
  const int rotw = qt % 12;
#define WFR(c, j) ld16(wv + wid * SLOT + ((c) * 12 + ((j) + rotw) % 12) * 1024 + lane * 16)
  const int dvo = lane * 16 + w4 * 6144, dq = w4 * 6144;

  if (wid >= 8) {
    const char* kv = ws + WS_H1 + (size_t)f * NP * ROWB;
#define KVH(k) (kv + ((qt + (k)) % 23) * SLOT)
#define BAR() do { __builtin_amdgcn_s_barrier(); asm volatile("" ::: "memory"); } while (0)
    if (w4 == 0 && lane < 16) LDSU(K2_FL + 4 * lane) = 0u;
    lds_barrier();
    dma6(KVH(0), dq, dvo); dma6(KVH(1), SLOT + dq, dvo);
    VMWAIT(0); BAR();
    dma6(KVH(2), 2 * SLOT + dq, dvo); dma6(KVH(3), 3 * SLOT + dq, dvo);
    BAR();
    dma6(KVH(4), 4 * SLOT + dq, dvo);
    VMWAIT(6);
#pragma unroll
    for (int i = 1; i <= 12; ++i) {
      BAR();
      const int fb = ((i + 2) % 3) * 2 * SLOT;
      const unsigned fl = LDSU(K2_FL + 4 * (i - 1));
      if (i == 1) { dma6(KVH(5), 5 * SLOT + dq, dvo); dma6(KVH(6), dq, dvo); dma6(KVH(7), SLOT + dq, dvo); }
      if (i >= 2 && i <= 11) {
        dma6(i <= 9 ? KVH(2 * i + 4) : wv + (2 * i - 16) * SLOT, fb + dq, dvo);
        if (i != 9) dma6(i <= 8 ? KVH(2 * i + 5) : wv + (2 * i - 15) * SLOT, fb + SLOT + dq, dvo);
      }
      if (i >= 2 && i <= 9) {
#pragma unroll
        for (int j = 3 * (i - 2); j < 3 * (i - 1); ++j) wreg[j] = WFR(j / 12, j % 12);
      }
      if (i >= 2) { if (RARE(__builtin_amdgcn_readfirstlane(fl))) { BAR(); BAR(); } }
      if (i == 1 || i == 10) VMWAIT(12); else if (i == 9) VMWAIT(9); else if (i < 9) VMWAIT(15);
    }
    VMWAIT(0);
  } else {
  {
    const char* wp = ws + WS_W1 + (size_t)(wid * 108 + 72) * 1024 + lane * 16;
#define QW(s) ld16(wp + ((((s) / 3 + rotw) % 12) * 3 + (s) % 3) * 1024)
#define QH(u, jk) (*(const bf16x8*)(sm + 4 * SLOT + (16 * (u) + r16) * ROWB + 16 * ((4 * (((jk) + rotw) % 12) + g) ^ swz(r16))))
    bf16x8 w[12], hb[2][2];
    f32x4 acc[3][2] = {};
    asm volatile("s_mov_b32 m0, %2\n\ts_nop 0\n\tglobal_load_lds_dwordx4 %0, %1\n\tglobal_load_lds_dwordx4 %0, %1 offset:1024\n\t"
                 "global_load_lds_dwordx4 %0, %1 offset:2048"
                 :: "v"(lane * 16 + wid * 3072), "s"(ws + WS_H1 + (size_t)(f * NP + 32 * qt) * ROWB), "s"(4 * SLOT + wid * 3072) : "memory");
    lds_barrier();
#pragma unroll
    for (int s = 0; s < 6; ++s) w[s] = QW(s);
    VMWAIT(6);
    if (lane == 0) __hip_atomic_fetch_add((LDS_AS unsigned*)(sm + K2_FL + 60), 1u, __ATOMIC_RELAXED, __HIP_MEMORY_SCOPE_WORKGROUP);
#pragma unroll
    for (int s = 6; s < 12; ++s) w[s] = QW(s);
    while (__builtin_amdgcn_readfirstlane(LDSU(K2_FL + 60)) < 8u) __builtin_amdgcn_s_sleep(1);
    asm volatile("" ::: "memory");
    hb[0][0] = QH(0, 0); hb[0][1] = QH(1, 0);
#pragma unroll
    for (int jk = 0; jk < 12; ++jk) {
      if (jk < 11) { hb[(jk + 1) & 1][0] = QH(0, jk + 1); hb[(jk + 1) & 1][1] = QH(1, jk + 1); }
#pragma unroll
      for (int c = 0; c < 3; ++c) {
        const int s = jk * 3 + c;
        const bf16x8 wf = w[s % 12];
        if (s + 12 < 36) w[s % 12] = QW(s + 12);
        acc[c][0] = MFMA16(wf, hb[jk & 1][0], acc[c][0]); acc[c][1] = MFMA16(wf, hb[jk & 1][1], acc[c][1]);
        SB0();
      }
    }
#pragma unroll
    for (int c = 0; c < 3; ++c) {
      char* qp = sm + 5 * SLOT + r16 * ROWB + 16 * ((6 * wid + 2 * c + (g >> 1)) ^ swz(r16)) + 8 * (g & 1);
      *(bf16x4*)qp = pack4(acc[c][0]); *(bf16x4*)(qp + 16 * ROWB) = pack4(acc[c][1]);
    }
  }
  if (wid < 4) {
    __builtin_amdgcn_s_setprio(2);
    FRESH_LANE();
    const int fsw = swz(r16);
    int roff[2][4];
#pragma unroll
    for (int b = 0; b < 4; ++b) { roff[0][b] = (16 * w4 + r16) * ROWB + 16 * ((4 * b + g) ^ fsw); roff[1][b] = roff[0][b] + 4 * SLOT; }
#define KFRAG(tbuf, ks) (*(const bf16x8*)(sm + roff[(tbuf) >> 1][(ks) & 3] + ((tbuf) & 1) * 2 * SLOT + ((ks) >> 2) * 256))
    const int pw_off = K2_P + r16 * 128 + 16 * ((2 * w4 + (g >> 1)) ^ (r16 & 7)) + 8 * (g & 1);
    const int kpad = (w4 & 1) ? 22 - qt : -1;
    bf16x8 qf[2][12];
    float R[2], l[2] = {0.f, 0.f}, ls[2];
    f32x4 s[2];
    auto mask_u = [&](int i, int u) {
      const int kidx = 2 * i + (w4 >> 1);
      if (RARE(kidx >= 23 || kidx == kpad)) {
#pragma unroll
        for (int r = 0; r < 4; ++r) s[u][r] = (kidx >= 23 || 4 * g + r >= 9) ? NEG : s[u][r];
      }
    };
    auto put_p = [&](int i) {
      f32x4 p[2];
#pragma unroll
      for (int r = 0; r < 4; ++r) { p[0][r] = __builtin_amdgcn_exp2f(s[0][r]); p[1][r] = __builtin_amdgcn_exp2f(s[1][r]); }
      *(bf16x4*)(sm + (i & 1) * 4096 + pw_off) = pack4(p[0]); *(bf16x4*)(sm + (i & 1) * 4096 + 2048 + pw_off) = pack4(p[1]);
      ls[0] = (p[0][0] + p[0][1]) + (p[0][2] + p[0][3]); ls[1] = (p[1][0] + p[1][1]) + (p[1][2] + p[1][3]);
    };
    auto put_max = [&]() {
      const float m0 = xm32(xm16(vmax(vmax3(s[0][0], s[0][1], s[0][2]), s[0][3]))), m1 = xm32(xm16(vmax(vmax3(s[1][0], s[1][1], s[1][2]), s[1][3])));
      if (g == 0) { *(float*)(sm + K2_MX + r16 * 16 + w4 * 4) = m0; *(float*)(sm + K2_MX + (16 + r16) * 16 + w4 * 4) = m1; }
    };
    auto get_max = [&](float (&M)[2]) {
#pragma unroll
      for (int u = 0; u < 2; ++u) {
        const f32x4 mm = *(const f32x4*)(sm + K2_MX + (16 * u + r16) * 16);
        M[u] = vmax(vmax3(mm[0], mm[1], mm[2]), mm[3]);
      }
    };
    auto redo = [&](int i) {
      put_max();
      lds_barrier();
      float M[2], a[2]; get_max(M);
#pragma unroll
      for (int u = 0; u < 2; ++u) { const float d = vmax(M[u], 0.f); a[u] = __builtin_amdgcn_exp2f(-d); R[u] += d; l[u] *= a[u]; s[u] -= d; }
      put_p(i - 1);
      if (wid == 0 && g == 0) { *(float*)(sm + K2_AL + r16 * 4) = a[0]; *(float*)(sm + K2_AL + 64 + r16 * 4) = a[1]; }
      lds_barrier();
    };

    lds_barrier();
    {
      bf16x8 kf[12];
#pragma unroll
      for (int ks = 0; ks < 12; ++ks) {
        kf[ks] = *(const bf16x8*)(sm + roff[0][ks & 3] + (ks >> 2) * 256);
        qf[0][ks] = *(const bf16x8*)(sm + roff[1][ks & 3] - 16 * w4 * ROWB + SLOT + (ks >> 2) * 256);
        qf[1][ks] = *(const bf16x8*)(sm + roff[1][ks & 3] - 16 * w4 * ROWB + SLOT + 16 * ROWB + (ks >> 2) * 256);
      }
      s[0] = f32x4{}; s[1] = f32x4{};
#pragma unroll
      for (int ks = 0; ks < 12; ++ks) { s[0] = MFMA16(kf[ks], qf[0][ks], s[0]); s[1] = MFMA16(kf[ks], qf[1][ks], s[1]); }
      mask_u(0, 0); mask_u(0, 1);
      put_max();
    }
    lds_barrier();
    get_max(R);
    s[0] -= R[0]; s[1] -= R[1];
    put_p(0);
#pragma unroll
    for (int i = 1; i <= 11; ++i) {
      lds_barrier();
      bf16x8 kf[6];
      const unsigned fl = LDSU(K2_FL + 4 * (i - 1));
#pragma unroll
      for (int ks = 0; ks < 6; ++ks) kf[ks] = KFRAG(i % 3, ks);
      if (i > 1) { if (RARE(__builtin_amdgcn_readfirstlane(fl))) redo(i); }
      l[0] += ls[0]; l[1] += ls[1];
      SB0();
      s[0] = f32x4{-R[0], -R[0], -R[0], -R[0]}; s[1] = f32x4{-R[1], -R[1], -R[1], -R[1]};
#pragma unroll
      for (int ks = 0; ks < 12; ++ks) {
        s[0] = MFMA16(kf[ks % 6], qf[0][ks], s[0]);
        s[1] = MFMA16(kf[ks % 6], qf[1][ks], s[1]);
        if (ks < 6) kf[ks] = KFRAG(i % 3, 6 + ks);
        if (i == 11) { wreg[ks] = WFR(0, ks); wreg[12 + ks] = WFR(1, ks); }
        SB0();
      }
      mask_u(i, 0); mask_u(i, 1);
      put_p(i);
      if (RARE(__any(!(ls[0] + ls[1] < T_BIG)))) { if (lane == 0) LDSU(K2_FL + 4 * i) = 1u; }
    }
    lds_barrier();
    if (RARE(__builtin_amdgcn_readfirstlane(LDSU(K2_FL + 4 * 11)))) redo(12);
#pragma unroll
    for (int u = 0; u < 2; ++u) {
      const float t = xsum32(xsum16(l[u] + ls[u]));
      if (g == 0) *(float*)(sm + K2_L + (16 * u + r16) * 16 + w4 * 4) = t;
    }
    __builtin_amdgcn_s_setprio(0);
  } else {
    FRESH_LANE();
    const int vsw = (8 * (g & 1)) | ((r16 >> 2) << 1);
    int voff[2][6];
#pragma unroll
    for (int i = 0; i < 6; ++i) {
      voff[0][i] = (8 * g + (r16 >> 2)) * ROWB + 16 * ((12 * w4 + 2 * i + ((lane & 3) >> 1)) ^ vsw) + 8 * (lane & 1);
      voff[1][i] = voff[0][i] + 3 * SLOT;
    }
    int pr_off[2];
#pragma unroll
    for (int hh = 0; hh < 2; ++hh) pr_off[hh] = K2_P + r16 * 128 + 16 * ((4 * hh + g) ^ (r16 & 7));
    f32x4 o[6][2];
#pragma unroll
    for (int i = 0; i < 6; ++i) { o[i][0] = f32x4{}; o[i][1] = f32x4{}; }
    bf16x8 vf[2][6];
    auto ld_v = [&](int tbuf, int hh, int c) {
      const int sl = 2 * tbuf + hh;
      return cat8(tr_read(sm + voff[sl / 3][c] + (sl % 3) * SLOT), tr_read(sm + voff[sl / 3][c] + (sl % 3) * SLOT + 4 * ROWB));
    };
    auto redo_wait = [&]() {
      lds_barrier(); lds_barrier();
      const float a0 = *(const float*)(sm + K2_AL + r16 * 4), a1 = *(const float*)(sm + K2_AL + 64 + r16 * 4);
#pragma unroll
      for (int c = 0; c < 6; ++c) { o[c][0] *= a0; o[c][1] *= a1; }
    };
    lds_barrier();
#pragma unroll
    for (int c = 0; c < 6; ++c) { vf[0][c] = ld_v(0, 0, c); vf[1][c] = ld_v(0, 1, c); }
    lds_barrier();
#pragma unroll
    for (int i = 1; i <= 11; ++i) {
      lds_barrier();
      const int sp = ((i - 1) & 1) * 4096;
      const unsigned fl = LDSU(K2_FL + 4 * (i - 1));
      bf16x8 pf[2][2];
#pragma unroll
      for (int hh = 0; hh < 2; ++hh) { pf[hh][0] = *(const bf16x8*)(sm + sp + pr_off[hh]); pf[hh][1] = *(const bf16x8*)(sm + sp + pr_off[hh] + 2048); }
      if (i > 1) {
        if (RARE(__builtin_amdgcn_readfirstlane(fl))) {
          redo_wait();
#pragma unroll
          for (int hh = 0; hh < 2; ++hh) { pf[hh][0] = *(const bf16x8*)(sm + sp + pr_off[hh]); pf[hh][1] = *(const bf16x8*)(sm + sp + pr_off[hh] + 2048); }
        }
      }
      SB0();
#pragma unroll
      for (int hh = 0; hh < 2; ++hh)
#pragma unroll
        for (int c = 0; c < 6; ++c) {
          o[c][0] = MFMA16(vf[hh][c], pf[hh][0], o[c][0]);
          o[c][1] = MFMA16(vf[hh][c], pf[hh][1], o[c][1]);
          if (i < 11 || hh == 0) vf[hh][c] = ld_v(i % 3, hh, c);
          SB0();
        }
    }
    lds_barrier();
    if (RARE(__builtin_amdgcn_readfirstlane(LDSU(K2_FL + 4 * 11)))) redo_wait();
    {
      const bf16x8 p0 = *(const bf16x8*)(sm + 4096 + pr_off[0]), p1 = *(const bf16x8*)(sm + 4096 + pr_off[0] + 2048);
#pragma unroll
      for (int c = 0; c < 6; ++c) { o[c][0] = MFMA16(vf[0][c], p0, o[c][0]); o[c][1] = MFMA16(vf[0][c], p1, o[c][1]); }
    }
    int lanee = __builtin_amdgcn_mbcnt_hi(-1, __builtin_amdgcn_mbcnt_lo(-1, 0));
    asm volatile("" : "+v"(lanee));
    const int ge = lanee >> 4, r16e = lanee & 15;
#pragma unroll
    for (int c = 0; c < 6; ++c) {
      char* op = sm + 5 * SLOT + r16e * ROWB + 16 * ((12 * w4 + 2 * c + (ge >> 1)) ^ swz(r16e)) + 8 * (ge & 1);
      *(bf16x4*)op = pack4(o[c][0]); *(bf16x4*)(op + 16 * ROWB) = pack4(o[c][1]);
    }
  }
  }

  int lanet = __builtin_amdgcn_mbcnt_hi(-1, __builtin_amdgcn_mbcnt_lo(-1, 0));
  asm volatile("" : "+v"(lanet));
  const int gt = lanet >> 4, r16t = lanet & 15;
  bf16x4 xres[2][2];
  float2 st[2];
#pragma unroll
  for (int u = 0; u < 2; ++u) {
    const int q = 32 * qt + 16 * u + r16t;
    const char* xp = ws + WS_H1 + (size_t)(f * NP + q) * ROWB + 8 * (gt & 1);
    xres[0][u] = *(const bf16x4*)(xp + 16 * ((4 * wid + (gt >> 1)) ^ swz(r16t))); xres[1][u] = *(const bf16x4*)(xp + 16 * ((4 * wid + 2 + (gt >> 1)) ^ swz(r16t)));
    st[u] = *(const float2*)(ws + WS_ST + (size_t)(f * NP + (q < NN ? q : 0)) * 8);
  }
  int ooff[4];
#pragma unroll
  for (int b = 0; b < 4; ++b) ooff[b] = 5 * SLOT + r16t * ROWB + 16 * ((4 * b + gt) ^ swz(r16t));
  lds_barrier();
  f32x4 acc[2][2] = {};
  float inv[2];
#pragma unroll
  for (int u = 0; u < 2; ++u) {
    const f32x4 ll = *(const f32x4*)(sm + K2_L + (16 * u + r16t) * 16);
    inv[u] = __builtin_amdgcn_rcpf((ll[0] + ll[1]) + (ll[2] + ll[3]));
  }
#define OFRAG(u, ks) (*(const bf16x8*)(sm + ooff[(ks) & 3] + (u) * 16 * ROWB + ((ks) >> 2) * 256))
  if (wid >= 4 && wid < 8) {
    const char* wl = sm + w4 * SLOT + lanet * 16;
#pragma unroll
    for (int ks = 0; ks < 12; ++ks) {
      const bf16x8 o0 = OFRAG(0, ks), o1 = OFRAG(1, ks), w0 = *(const bf16x8*)(wl + ks * 1024), w1 = *(const bf16x8*)(wl + (12 + ks) * 1024);
      acc[0][0] = MFMA16(w0, o0, acc[0][0]); acc[0][1] = MFMA16(w0, o1, acc[0][1]);
      acc[1][0] = MFMA16(w1, o0, acc[1][0]); acc[1][1] = MFMA16(w1, o1, acc[1][1]);
    }
  } else {
#pragma unroll
    for (int ks = 0; ks < 12; ++ks) {
      const char* ob = sm + 5 * SLOT + r16t * ROWB + 16 * ((4 * ((ks + rotw) % 12) + gt) ^ swz(r16t));
      const bf16x8 o0 = *(const bf16x8*)ob, o1 = *(const bf16x8*)(ob + 16 * ROWB);
      acc[0][0] = MFMA16(wreg[ks], o0, acc[0][0]); acc[0][1] = MFMA16(wreg[ks], o1, acc[0][1]);
      acc[1][0] = MFMA16(wreg[12 + ks], o0, acc[1][0]); acc[1][1] = MFMA16(wreg[12 + ks], o1, acc[1][1]);
    }
  }
#pragma unroll
  for (int u = 0; u < 2; ++u) {
    const int q = 32 * qt + 16 * u + r16t;
    if (q < NN) {
      char* op = out + (size_t)(f * NN + q) * ROWB + (32 * wid + 4 * gt) * 2;
#pragma unroll
      for (int c = 0; c < 2; ++c) {
        f32x4 v = acc[c][u];
#pragma unroll
        for (int r = 0; r < 4; ++r) v[r] = v[r] * inv[u] + ((float)xres[c][u][r] * st[u].y + st[u].x);
        *(bf16x4*)(op + c * 32) = pack4(v);
      }
    }
  }
}

}

extern "C" void kernel_launch(void* const* d_in, const int* in_sizes, int n_in,
                              void* d_out, int out_size, void* d_ws, size_t ws_size,
                              hipStream_t stream) {
  (void)in_sizes; (void)n_in; (void)out_size; (void)ws_size;
  const char* x = (const char*)d_in[0];
  char* ws = (char*)d_ws;
  (void)hipFuncSetAttribute((const void*)k2_spatial, hipFuncAttributeMaxDynamicSharedMemorySize, K2_LDS);
  k0_fold<<<256, 256, 0, stream>>>((const char*)d_in[1], (const char*)d_in[2], (const char*)d_in[3], (const char*)d_in[4], ws);
  k1_temporal<<<183, 512, 0, stream>>>(x, ws);
  k2_spatial<<<184, 768, K2_LDS, stream>>>(ws, (char*)d_out);
}
```
